```python
import torch

def _softmax(x, axis=-1):
    return torch.softmax(x, dim=axis)

def _gelu(x):
    return torch.nn.functional.gelu(x, approximate='tanh')

def _silu(x):
    return torch.nn.functional.silu(x)

def _sigmoid(x):
    return torch.sigmoid(x)

def _layernorm(x, axis=-1, eps=1e-05):
    m = x.mean(dim=axis, keepdim=True)
    v = x.var(dim=axis, keepdim=True, unbiased=False)
    return (x - m) / torch.sqrt(v + eps)

def _scatter_add(tgt, idx, src, axis=0):
    return tgt.clone().index_add_(axis, idx, src)

def _segment_sum(src, seg, n):
    out = torch.zeros((n,) + src.shape[1:], dtype=src.dtype)
    return out.index_add_(0, seg, src)

def _pad(x, pw):
    flat = tuple((v for pair in reversed(pw) for v in pair))
    return torch.nn.functional.pad(x, flat)
kernel_name = 'recurrent_scan__memory_aug_recurrent_mem_tr__glu_gated_swiglu__prompt'

def setup_inputs():

    _w = lambda a: (lambda t: t.to(torch.bfloat16) if t.is_floating_point() else t)(torch.as_tensor(a))

    def _A_setup():
        B, S, Dm = (1, 2048, 448)
        H, Dh = (16, 256)
        M, seg = (8, 32)
        seg = min(seg, S)
        S = S // seg * seg
        return {'x': _w((lambda *s: torch.randn(*s, dtype=torch.bfloat16) * 0.1)(B, S, Dm)), 'mem0': _w((lambda *s: torch.randn(*s, dtype=torch.bfloat16) * 1.6)(B, M, Dm)), 'Wq': _w((lambda *s: torch.randn(*s, dtype=torch.bfloat16) * 0.8)(Dm, H, Dh) * Dm ** (-0.5)), 'Wk': _w((lambda *s: torch.randn(*s, dtype=torch.bfloat16) * 0.8)(Dm, H, Dh) * Dm ** (-0.5)), 'Wv': _w((lambda *s: torch.randn(*s, dtype=torch.bfloat16) * 1.6)(Dm, H, Dh) * Dm ** (-0.5)), 'Wo': _w((lambda *s: torch.randn(*s, dtype=torch.bfloat16) * 1.6)(H, Dh, Dm) * (H * Dh) ** (-0.5)), 'seg_len': seg}
    _A_in = _A_setup()

    def _B_setup():
        B, S, D, F = (1, 2048, 448, 1792)
        return {'x': _w((lambda *s: torch.randn(*s, dtype=torch.bfloat16) * 0.1)(B, S, D)), 'w1': _w((lambda *s: torch.randn(*s, dtype=torch.bfloat16) * 0.4)(D, F) * (2.0 / D) ** 0.5), 'w3': _w((lambda *s: torch.randn(*s, dtype=torch.bfloat16) * 0.4)(D, F) * (2.0 / D) ** 0.5), 'w2': _w((lambda *s: torch.randn(*s, dtype=torch.bfloat16) * 0.4)(F, D) * (2.0 / F) ** 0.5)}
    _B_in = _B_setup()
    return {'A_x': _A_in['x'], 'A_mem0': _A_in['mem0'], 'A_Wq': _A_in['Wq'], 'A_Wk': _A_in['Wk'], 'A_Wv': _A_in['Wv'], 'A_Wo': _A_in['Wo'], 'A_seg_len': _A_in['seg_len'], 'B_x': _B_in['x'], 'B_w1': _B_in['w1'], 'B_w3': _B_in['w3'], 'B_w2': _B_in['w2']}

def reference(A_x, A_mem0, A_Wq, A_Wk, A_Wv, A_Wo, A_seg_len, B_x, B_w1, B_w3, B_w2):

    def _A_ref(x, mem0, Wq, Wk, Wv, Wo, seg_len):
        B, S, Dm = x.shape
        H, Dh = (Wq.shape[1], Wq.shape[2])
        M = mem0.shape[1]
        scale = Dh ** (-0.5)
        n_seg = S // seg_len
        L = M + seg_len
        idx = torch.arange(L)
        mask = (idx[:, None] < M) | (idx[:, None] >= idx[None, :])
        mem = mem0
        outs = []
        for s in range(n_seg):
            xs = x[:, s * seg_len:(s + 1) * seg_len, :].clone()
            h = torch.concatenate([mem, xs], axis=1)
            q = torch.einsum('btd,dhe->bhte', h, Wq)
            k = torch.einsum('btd,dhe->bhte', h, Wk)
            v = torch.einsum('btd,dhe->bhte', h, Wv)
            scores = torch.einsum('bhqe,bhke->bhqk', q, k) * scale
            scores = torch.where(mask[None, None], scores, -1000000000.0)
            p = _softmax(scores, axis=-1)
            o = torch.einsum('bhqk,bhke->bhqe', p, v)
            o = torch.einsum('bhte,hed->btd', o, Wo)
            mem = o[:, :M, :].clone()
            outs.append(o[:, M:, :])
        return torch.concatenate(outs, axis=1)

    def _B_ref(x, w1, w3, w2):
        gate = torch.einsum('bsd,df->bsf', x, w1)
        up = torch.einsum('bsd,df->bsf', x, w3)
        h = _silu(gate) * up
        out = torch.einsum('bsf,fd->bsd', h, w2)
        return out
    x = A_x
    xn = _layernorm(x, axis=-1)
    s = _A_ref(xn, A_mem0, A_Wq, A_Wk, A_Wv, A_Wo, A_seg_len)
    g = _silu(_B_ref(xn, B_w1, B_w3, B_w2))
    return x + s * g
```

```cpp
#include <hip/hip_runtime.h>
#include <hip/hip_bf16.h>
#include <stdint.h>

namespace {

typedef unsigned int u32;
typedef unsigned short u16;
typedef unsigned long long u64;
typedef u32 v4u __attribute__((ext_vector_type(4)));
typedef u32 v2u __attribute__((ext_vector_type(2)));
typedef float v4f __attribute__((ext_vector_type(4)));
typedef float v2f __attribute__((ext_vector_type(2)));
typedef short v4s __attribute__((ext_vector_type(4)));
typedef __bf16 v8bf __attribute__((ext_vector_type(8)));
typedef __bf16 v2bf __attribute__((ext_vector_type(2)));
#define LAS __attribute__((address_space(3)))
#define GAS __attribute__((address_space(1)))
#define DI __device__ __forceinline__
typedef __amdgpu_buffer_rsrc_t rsrc_t;

constexpr int NSEG = 64, DM = 448, NH = 16, FF = 1792, KS = 14;
constexpr float SCL = 1.4426950408889634f / 16.0f;
constexpr float NEGM = -1.0e9f * 1.4426950408889634f;

constexpr u32 STEP = 32768;
constexpr u32 H_MB = 0, H_SB = 8192, H_EB = 16384, H_MX = 24576, H_END = 26624;
constexpr u32 OFF_HAND = 0;
constexpr u32 OFF_FLAG = 4u << 20;
constexpr u32 F_WPROG = OFF_FLAG, F_ZPROG = F_WPROG + 168 * 128, F_HCNT = F_ZPROG + 56 * 128, F_PCNT = F_HCNT + 32 * 128,
              F_TMO = F_PCNT + 64 * 128, F_END = F_TMO + 128;
constexpr u32 OFF_DBG = OFF_FLAG + (64u << 10);
constexpr u32 OFF_XN = 5u << 20;
constexpr u32 OFF_WB = OFF_XN + 128 * KS * 1024;
constexpr u32 WBSZ = 8 * KS * 1024;
constexpr u32 OFF_GT = OFF_WB + 168 * WBSZ;
constexpr u32 OFF_U = OFF_GT + NSEG * NH * 56 * 1024;
constexpr u32 OFF_H = OFF_U + NSEG * 56 * 8192;
constexpr u32 OFF_PX = OFF_H + 128 * 56 * 1024;
constexpr u32 OFF_PM = OFF_PX + NSEG * NH * 2048;
constexpr u32 OFF_Z = OFF_PM + NSEG * 4 * 2048;
constexpr u32 WS_END = OFF_Z + NSEG * 56 * 2048;

constexpr int LDS_MAIN = 146 * 1024;
constexpr int LDS_PREP = 120 * 1024;
constexpr u32 SPIN_CAP = 1u << 18;

struct Args {
  const u16* x; const u16* mem0; const u16* wq; const u16* wk; const u16* wv; const u16* wo;
  const u16* w1; const u16* w3; const u16* w2; u16* out; char* ws; long long mode;
};

DI v4f mfma32(v4u a, v4u b, v4f c) {
  return __builtin_amdgcn_mfma_f32_16x16x32_bf16(__builtin_bit_cast(v8bf, a), __builtin_bit_cast(v8bf, b), c, 0, 0, 0);
}
DI v4f mfma16(v2u a, v2u b, v4f c) {
  return __builtin_amdgcn_mfma_f32_16x16x16bf16_1k(__builtin_bit_cast(v4s, a), __builtin_bit_cast(v4s, b), c, 0, 0, 0);
}
DI u32 pk(float lo, float hi) { v2f t = {lo, hi}; v2bf b = __builtin_convertvector(t, v2bf); return __builtin_bit_cast(u32, b); }
DI float rbf(float x) { return __uint_as_float(pk(x, 0.f) << 16); }
DI float bflo(u32 v) { return __uint_as_float(v << 16); }
DI float bfhi(u32 v) { return __uint_as_float(v & 0xffff0000u); }
DI u32 canon(u32 v) { return v < 0xfffffffeu ? v : 0xfffffffeu; }
DI float ex2(float x) { return __builtin_amdgcn_exp2f(x); }
DI float rcp(float x) { return __builtin_amdgcn_rcpf(x); }
DI float silu(float x) { return x / (1.0f + __expf(-x)); }
DI int uni(int v) { return __builtin_amdgcn_readfirstlane(v); }
DI void bar() { asm volatile("s_waitcnt lgkmcnt(0)\n\ts_barrier" ::: "memory"); }
DI void drain() { asm volatile("s_waitcnt vmcnt(0)" ::: "memory"); }
DI void cfence() { asm volatile("" ::: "memory"); }
DI v4u ld_dev(rsrc_t r, u32 v, u32 s) { return __builtin_amdgcn_raw_buffer_load_b128(r, v, s, 16); }
DI u32 ld_dev1(rsrc_t r, u32 v, u32 s) { return __builtin_amdgcn_raw_buffer_load_b32(r, v, s, 16); }
DI v4u ld_pl(rsrc_t r, u32 v, u32 s) { return __builtin_amdgcn_raw_buffer_load_b128(r, v, s, 0); }
DI void st_dev(v4u d, rsrc_t r, u32 v, u32 s) { __builtin_amdgcn_raw_buffer_store_b128(d, r, v, s, 16); }
DI void st_dev1(u32 d, rsrc_t r, u32 v, u32 s) { __builtin_amdgcn_raw_buffer_store_b32(d, r, v, s, 16); }
DI void st_pl(v4u d, rsrc_t r, u32 v, u32 s) { __builtin_amdgcn_raw_buffer_store_b128(d, r, v, s, 0); }
DI v4u lds_r128(char* l, u32 o) { return *(LAS v4u*)(l + o); }
DI v2u lds_r64(char* l, u32 o) { return *(LAS v2u*)(l + o); }
DI void lds_w128(char* l, u32 o, v4u v) { *(LAS v4u*)(l + o) = v; }
DI void lds_w64(char* l, u32 o, v2u v) { *(LAS v2u*)(l + o) = v; }
DI void lds_w32(char* l, u32 o, u32 v) { *(LAS u32*)(l + o) = v; }
DI v2u lds_tr(char* l, u32 o) { return __builtin_bit_cast(v2u, __builtin_amdgcn_ds_read_tr16_b64_v4i16((LAS v4s*)(l + o))); }
DI v4u f2u(v4f v) { return __builtin_bit_cast(v4u, v); }
DI v4f u2f(v4u v) { return __builtin_bit_cast(v4f, v); }
DI float xmax16(float v) { u32 u = __float_as_uint(v); auto s = __builtin_amdgcn_permlane16_swap(u, u, false, false); return fmaxf(__uint_as_float(s[0]), __uint_as_float(s[1])); }
DI float xmax32(float v) { u32 u = __float_as_uint(v); auto s = __builtin_amdgcn_permlane32_swap(u, u, false, false); return fmaxf(__uint_as_float(s[0]), __uint_as_float(s[1])); }
DI float xadd16(float v) { u32 u = __float_as_uint(v); auto s = __builtin_amdgcn_permlane16_swap(u, u, false, false); return __uint_as_float(s[0]) + __uint_as_float(s[1]); }
DI float xadd32(float v) { u32 u = __float_as_uint(v); auto s = __builtin_amdgcn_permlane32_swap(u, u, false, false); return __uint_as_float(s[0]) + __uint_as_float(s[1]); }
DI u32 ror8(u32 v) { return (u32)__builtin_amdgcn_update_dpp(0, (int)v, 0x128, 0xf, 0xf, true); }
DI float ror8f(float v) { return __uint_as_float(ror8(__float_as_uint(v))); }
DI bool unset(v4u v) { u32 m = v.x > v.y ? v.x : v.y; m = m > v.z ? m : v.z; m = m > v.w ? m : v.w; return m == 0xffffffffu; }
DI bool any(bool p) { return __builtin_amdgcn_ballot_w64(p) != 0ull; }
DI void glds16(const void* gsrc, u32 lds_dst) {
  u32 keep;
  asm volatile("s_mov_b32 %0, m0\n\ts_mov_b32 m0, %2\n\ts_nop 0\n\tglobal_load_lds_dwordx4 %1, off\n\ts_mov_b32 m0, %0"
               : "=&s"(keep) : "v"(gsrc), "s"(lds_dst) : "memory");
}
DI void give_up(rsrc_t ws, bool& dead) { dead = true; st_dev1(1u, ws, F_TMO, 0); }

template <int NL>
DI u32 wait_flags(rsrc_t ws, u32 base, u32 want, int lane, bool& dead) {
  const u32 vo = (u32)(lane < NL ? lane : 0) * 128u;
  for (u32 it = 0;; ++it) {
    cfence();
    const u32 v = ld_dev1(ws, vo, base);
    u32 mn = (u32)__builtin_amdgcn_readlane((int)v, 0);
#pragma unroll
    for (int l = 1; l < NL; ++l) { const u32 t = (u32)__builtin_amdgcn_readlane((int)v, l); mn = t < mn ? t : mn; }
    if (mn >= want) return mn;
    if (dead) return want;
    if (it > SPIN_CAP) { give_up(ws, dead); return want; }
    __builtin_amdgcn_s_sleep(4);
  }
}
DI v4u poll16(rsrc_t ws, u32 vo, u32 so, bool& dead) {
  for (u32 it = 0;; ++it) {
    cfence();
    const v4u v = ld_dev(ws, vo, so);
    if (!any(unset(v)) || dead) return v;
    if (it > SPIN_CAP) { give_up(ws, dead); return v; }
    __builtin_amdgcn_s_sleep(1);
  }
}

constexpr u32 SP = 144, SIMG = DM * SP;
DI void stage64(const u16* p0, const u16* p1, int rstride, char* lds, int tid) {
  v4u t[14];
#pragma unroll
  for (int it = 0; it < 14; ++it) {
    const int row = (it % 7) * 64 + (tid >> 3);
    t[it] = *(const GAS v4u*)((it < 7 ? p0 : p1) + (size_t)row * rstride + (tid & 7) * 8);
  }
#pragma unroll
  for (int it = 0; it < 14; ++it) {
    const int row = (it % 7) * 64 + (tid >> 3);
    lds_w128(lds, (it < 7 ? 0u : SIMG) + row * SP + (tid & 7) * 16, t[it]);
  }
}
DI v4u tr_frag(char* lds, u32 base, u32 pitch, int ks, int lane, u32 piece) {
  const u32 g = lane >> 4, q4 = (lane & 15) >> 2;
  const u32 o = base + (32 * ks + 8 * g + q4) * pitch + piece;
  const v2u a = lds_tr(lds, o), b = lds_tr(lds, o + 4 * pitch);
  return (v4u){a.x, a.y, b.x, b.y};
}

__global__ void __launch_bounds__(512, 2) k_prep(Args a) {
  extern __shared__ __attribute__((aligned(16))) char lds[];
  const int tid = threadIdx.x, lane = tid & 63, w = uni(tid >> 6), r = lane & 15, g = lane >> 4;
  const rsrc_t ws = __builtin_amdgcn_make_buffer_rsrc(a.ws, 0, WS_END, 0x00020000);
  const int b = blockIdx.x;
  if (b >= 192) {
    const int s = b - 192;
    const v4u ones = {~0u, ~0u, ~0u, ~0u};
    for (u32 o = tid * 16; o < H_END; o += 8192) {
      v4u v = ones;
      if (s == 0 && o < 7168) {
        const u32 cq = o >> 7, i = (o >> 4) & 7;
        v = *(const GAS v4u*)(a.mem0 + i * DM + cq * 8);
        v.x = canon(v.x); v.y = canon(v.y); v.z = canon(v.z); v.w = canon(v.w);
      }
      st_pl(v, ws, OFF_HAND + s * STEP + o, 0);
    }
    if (s == 1) for (u32 o = tid * 16; o < F_END - OFF_FLAG; o += 8192) st_pl((v4u){0, 0, 0, 0}, ws, OFF_FLAG + o, 0);
    const int le = lane < 56 ? lane : 55;
    v4u xv[4];
#pragma unroll
    for (int i = 0; i < 4; ++i) xv[i] = *(const GAS v4u*)(a.x + (size_t)(s * 32 + w * 4 + i) * DM + le * 8);
#pragma unroll
    for (int i = 0; i < 4; ++i) {
      float f[8] = {bflo(xv[i].x), bfhi(xv[i].x), bflo(xv[i].y), bfhi(xv[i].y), bflo(xv[i].z), bfhi(xv[i].z), bflo(xv[i].w), bfhi(xv[i].w)};
      float sum = 0.f;
#pragma unroll
      for (int k = 0; k < 8; ++k) sum += f[k];
      if (lane >= 56) sum = 0.f;
#pragma unroll
      for (int m = 1; m < 64; m <<= 1) sum += __shfl_xor(sum, m);
      const float mean = sum * (1.0f / DM);
      float sq = 0.f;
#pragma unroll
      for (int k = 0; k < 8; ++k) sq += (f[k] - mean) * (f[k] - mean);
      if (lane >= 56) sq = 0.f;
#pragma unroll
      for (int m = 1; m < 64; m <<= 1) sq += __shfl_xor(sq, m);
      const float mb = rbf(mean), den = rbf(sqrtf(rbf(rbf(sq * (1.0f / DM)) + 1e-5f)));
#pragma unroll
      for (int k = 0; k < 8; ++k) f[k] = rbf(f[k] - mb) / den;
      const v4u o = {pk(f[0], f[1]), pk(f[2], f[3]), pk(f[4], f[5]), pk(f[6], f[7])};
      const int t = w * 4 + i;
      if (lane < 56) st_pl(o, ws, OFF_XN + ((s * 2 + (t >> 4)) * KS + (le >> 2)) * 1024 + ((le & 3) * 16 + (t & 15)) * 16, 0);
    }
    return;
  }
  const int ty = b >> 6, h = (b & 63) >> 2, qd = b & 3;
  const u16* ksrc = ty == 0 ? a.wk : (ty == 1 ? a.wq : a.wv);
  const int kw = w < 7 ? w : 6;
  const u16* kp = ksrc + (size_t)(64 * kw + 8 * (r >> 2) + (r & 3)) * 4096 + h * 256 + g * 8;
  v4u kf[3][4];
#pragma unroll
  for (int e = 0; e < 2; ++e)
#pragma unroll
    for (int t = 0; t < 4; ++t) kf[e][t] = *(const GAS v4u*)(kp + (size_t)(32 * (t >> 1) + 4 * (t & 1)) * 4096 + e * 32);
  if (ty < 2) {
    const u16* nsrc = ty == 0 ? a.wq : a.wk;
    v4u t[7];
#pragma unroll
    for (int i = 0; i < 7; ++i) {
      const int ct = 7 * qd + i, n = 32 * (ct >> 1) + 8 * (r >> 2) + (r & 3) + 4 * (ct & 1);
      t[i] = *(const GAS v4u*)(nsrc + (size_t)n * 4096 + h * 256 + w * 32 + g * 8);
    }
#pragma unroll
    for (int i = 0; i < 7; ++i) lds_w128(lds, (i * 8 + w) * 1024 + lane * 16, t[i]);
  } else {
    constexpr u32 RAW = 56 * 1024, RP = 240;
    v4u t[7];
#pragma unroll
    for (int i = 0; i < 7; ++i) {
      const int c = i * 64 + lane, row = c / 14, ch = c % 14;
      t[i] = *(const GAS v4u*)(a.wo + (size_t)(h * 256 + w * 32 + row) * DM + qd * 112 + ch * 8);
    }
#pragma unroll
    for (int i = 0; i < 7; ++i) {
      const int c = i * 64 + lane, row = c / 14, ch = c % 14;
      lds_w128(lds, RAW + (w * 32 + row) * RP + ch * 16, t[i]);
    }
    asm volatile("s_waitcnt lgkmcnt(0)" ::: "memory");
#pragma unroll
    for (int i = 0; i < 7; ++i) t[i] = tr_frag(lds, RAW, RP, w, lane, i * 32 + (lane & 3) * 8);
#pragma unroll
    for (int i = 0; i < 7; ++i) lds_w128(lds, (i * 8 + w) * 1024 + lane * 16, t[i]);
  }
  bar();
  if (w == 7) return;
  v4f acc[4][7];
#pragma unroll
  for (int t = 0; t < 4; ++t)
#pragma unroll
    for (int i = 0; i < 7; ++i) acc[t][i] = (v4f){0, 0, 0, 0};
#pragma unroll
  for (int e = 0; e < 8; ++e) {
    if (e + 2 < 8) {
#pragma unroll
      for (int t = 0; t < 4; ++t) kf[(e + 2) % 3][t] = *(const GAS v4u*)(kp + (size_t)(32 * (t >> 1) + 4 * (t & 1)) * 4096 + (e + 2) * 32);
    }
#pragma unroll
    for (int i = 0; i < 7; ++i) {
      const v4u nf = lds_r128(lds, (i * 8 + e) * 1024 + lane * 16);
#pragma unroll
      for (int t = 0; t < 4; ++t) acc[t][i] = mfma32(kf[e % 3][t], nf, acc[t][i]);
    }
  }
  const float sc = ty < 2 ? SCL : 1.0f;
#pragma unroll
  for (int i = 0; i < 7; ++i) {
    const int ct28 = 7 * qd + i;
    u32 dst;
    if (ty < 2) {
      const int uu = ty * 14 + (ct28 >> 1);
      dst = (56 + 7 * h + (uu >> 2)) * WBSZ + (2 * (uu & 3) + (ct28 & 1)) * KS * 1024 + lane * 16;
    } else {
      dst = (2 * ct28 + (r >> 3)) * WBSZ + (h >> 1) * KS * 1024 + ((h & 1) * 8 + (r & 7) + 16 * g) * 16;
    }
#pragma unroll
    for (int k2 = 0; k2 < 2; ++k2) {
      const v4f lo = acc[2 * k2][i] * sc, hi = acc[2 * k2 + 1][i] * sc;
      st_pl((v4u){pk(lo[0], lo[1]), pk(lo[2], lo[3]), pk(hi[0], hi[1]), pk(hi[2], hi[3])}, ws, OFF_WB + dst + (2 * w + k2) * 1024, 0);
    }
  }
}

#define DLY_MEM 4
#define DLY_S 0
DI void role_chain(const Args& a, rsrc_t ws, int q, char* lds) {
  const int tid = threadIdx.x, lane = tid & 63, w = uni(tid >> 6), r = lane & 15, g = lane >> 4;
  const int h = q >> 2, j = q & 3;
  const bool outr = q < 56;
  bool dead = false;
  v4u wq[KS], wz[KS], mf[KS];
  stage64(a.wq + h * 256 + j * 64, a.wk + h * 256 + j * 64, 4096, lds, tid);
  if (outr) {
#pragma unroll
    for (int ks = 0; ks < KS; ++ks) wz[ks] = ld_pl(ws, lane * 16 + ks * 1024, OFF_WB + q * WBSZ + w * KS * 1024);
  } else {
#pragma unroll
    for (int ks = 0; ks < KS; ++ks) wz[ks] = (v4u){0, 0, 0, 0};
  }
  bar();
#pragma unroll
  for (int ks = 0; ks < KS; ++ks) wq[ks] = tr_frag(lds, (w >> 2) * SIMG, SP, ks, lane, (w & 3) * 32 + (lane & 3) * 8);
  bar();
  constexpr u32 L_MEM = 0, L_QK = 8192, L_Z = 12288, L_RED = 16384;
  const u32 rm = 2 * (r >> 2) + (r & 1);
  const u32 le = lane < 56 ? lane : 55;
  const u32 hh = r >> 3, i8 = r & 7;
  const u32 s1 = 2 * ((r >> 2) ^ ((r >> 1) & 1)) + (r & 1);
  v4u selS, selO;
#pragma unroll
  for (int d = 0; d < 4; ++d) {
    selS[d] = (s1 == 2 * d ? 0x3f80u : 0u) | (s1 == 2 * d + 1 ? 0x3f800000u : 0u);
    selO[d] = (i8 == 2 * d ? 0x3f80u : 0u) | (i8 == 2 * d + 1 ? 0x3f800000u : 0u);
  }
  const v4u one4 = {0x3f803f80u, 0x3f803f80u, 0x3f803f80u, 0x3f803f80u};
  const v2u one2 = {0x3f803f80u, 0u};
  const v4f zero = {0, 0, 0, 0};
  const u32 vo_hs = ((2 * w + hh) * 4 + g) * 128 + i8 * 16;
  const u32 vo_mx = (2 * w + hh) * 128 + i8 * 4;
  u32 uprog = 0;
  v4u uf = {0, 0, 0, 0};
  if (outr) {
    uprog = wait_flags<1>(ws, F_WPROG + q * 128, 1, lane, dead);
    uf = ld_dev(ws, lane * 16, OFF_U + q * 8192 + w * 1024);
  }
  for (int s = 0; s < NSEG; ++s) {
    const u32 hb = OFF_HAND + s * STEP;
    const bool last = s == NSEG - 1;
    if (last && !outr) break;
    {
      const v4u v = poll16(ws, (w * 56 + le) * 16, hb + H_MB, dead);
      lds_w128(lds, L_MEM + (w * 56 + le) * 16, v);
      if (s > 0 && w == 1 && outr && lane == 0) st_dev1((u32)s, ws, F_ZPROG + q * 128, 0);
    }
    bar();
#pragma unroll
    for (int ks = 0; ks < KS; ++ks) mf[ks] = lds_r128(lds, L_MEM + (4 * ks + g) * 128 + rm * 16);
    if (!last) {
      v4f acc = zero;
#pragma unroll
      for (int ks = 0; ks < KS; ++ks) acc = mfma32(wq[ks], mf[ks], acc);
      lds_w64(lds, L_QK + ((w >> 2) * 8 + rm) * SP + (w & 3) * 32 + g * 8, (v2u){pk(acc[0], acc[1]), pk(acc[2], acc[3])});
      bar();
      if (w == 0) {
        __builtin_amdgcn_s_setprio(3);
        const v4u q0 = lds_r128(lds, L_QK + i8 * SP + g * 16), q1 = lds_r128(lds, L_QK + i8 * SP + 64 + g * 16);
        const v4u k0 = lds_r128(lds, L_QK + (8 + i8) * SP + g * 16), k1 = lds_r128(lds, L_QK + (8 + i8) * SP + 64 + g * 16);
        v4f d = mfma32(k0, q0, zero);
        d = mfma32(k1, q1, d);
        d *= SCL;
        const u32 p0 = canon(pk(d[0], d[1])), p1 = canon(pk(d[2], d[3]));
        const auto t0 = __builtin_amdgcn_permlane16_swap(p0, p0, false, false);
        const auto t1 = __builtin_amdgcn_permlane16_swap(p1, p1, false, false);
        if (lane < 8) st_dev((v4u){p0, p1, t0[1], t1[1]}, ws, q * 128 + lane * 16, hb + H_SB);
        __builtin_amdgcn_s_setprio(0);
      }
    }
    if (outr) {
      v4f az = zero;
#pragma unroll
      for (int ks = 0; ks < KS; ++ks) az = mfma32(mf[ks], wz[ks], az);
      const u32 zp = pk(az[0], az[1]);
      lds_w32(lds, L_Z + (w * 16 + r) * 16 + g * 4, zp);
      if (!last) {
        v4u ev; u32 mxu;
        for (u32 it = 0;; ++it) {
          cfence();
          ev = ld_dev(ws, vo_hs, hb + H_EB);
          mxu = ld_dev1(ws, vo_mx, hb + H_MX);
          if (!any(unset(ev) || mxu == 0xffffffffu) || dead) break;
          if (it > SPIN_CAP) { give_up(ws, dead); break; }
          __builtin_amdgcn_s_sleep(1);
        }
        const v4f wv = mfma32(uf, ev, zero);
        const v4f lv = mfma32(one4, ev, zero);
        const float mx = __uint_as_float(mxu);
        if (DLY_S) __builtin_amdgcn_s_sleep(DLY_S);
        const v4u sv = poll16(ws, vo_hs, hb + H_SB, dead);
        const v4f sc = mfma32(selS, sv, zero);
        const float M = fmaxf(xmax32(fmaxf(fmaxf(sc[0], sc[1]), fmaxf(sc[2], sc[3]))), mx);
        const float f = ex2(mx - M);
        const v2u pe = {pk(ex2(sc[0] - M), ex2(sc[1] - M)), 0u};
        const v4f lr = mfma16(one2, pe, lv * f);
        const v4f d = mfma16((v2u){zp, 0u}, pe, wv * f) * rcp(lr[0]);
        if (hh == (u32)(g >> 1)) lds_w64(lds, L_RED + ((2 * w + hh) * 8 + i8) * 16 + (g & 1) * 8, (v2u){pk(d[0], d[1]), pk(d[2], d[3])});
      }
      bar();
      if (w == 0 && !last) {
        const v4u b0 = lds_r128(lds, L_RED + ((hh * 8 + g) * 8 + i8) * 16), b1 = lds_r128(lds, L_RED + ((hh * 8 + 4 + g) * 8 + i8) * 16);
        v4f d = mfma32(selO, b0, zero);
        d = mfma32(selO, b1, d);
        const float o0 = d[0] + ror8f(d[0]), o1 = d[1] + ror8f(d[1]), o2 = d[2] + ror8f(d[2]), o3 = d[3] + ror8f(d[3]);
        const u32 p0 = canon(pk(o0, o1)), p1 = canon(pk(o2, o3));
        const auto t0 = __builtin_amdgcn_permlane16_swap(p0, p0, false, false);
        const auto t1 = __builtin_amdgcn_permlane16_swap(p1, p1, false, false);
        if (lane < 8) st_dev((v4u){p0, p1, t0[1], t1[1]}, ws, q * 128 + lane * 16, hb + STEP + H_MB);
      }
      if (w == 1) {
        const v4u z0 = lds_r128(lds, L_Z + lane * 16), z1 = lds_r128(lds, L_Z + 1024 + lane * 16);
        st_dev(z0, ws, lane * 16, OFF_Z + (s * 56 + q) * 2048);
        st_dev(z1, ws, lane * 16 + 1024, OFF_Z + (s * 56 + q) * 2048);
        if (last) { drain(); if (lane == 0) st_dev1((u32)NSEG, ws, F_ZPROG + q * 128, 0); }
      }
      if (s + 2 < NSEG) {
        const u32 need = ((u32)(s + 1) >> 1) + 1;
        if (uprog < need) uprog = wait_flags<1>(ws, F_WPROG + q * 128, need, lane, dead);
        uf = ld_dev(ws, lane * 16, OFF_U + ((s + 1) * 56 + q) * 8192 + w * 1024);
      }
      if (DLY_MEM) __builtin_amdgcn_s_sleep(DLY_MEM);
    } else {
      __builtin_amdgcn_s_sleep(32);
    }
  }
}

DI void role_g(const Args& a, rsrc_t ws, int h, char* lds) {
  const int tid = threadIdx.x, lane = tid & 63, w = uni(tid >> 6), r = lane & 15, g = lane >> 4;
  bool dead = false;
  constexpr u32 L_MEM = 0, L_RED = 8192;
  const u32 le = lane < 56 ? lane : 55, i8 = r & 7, hi = r >> 3;
  const int tt = w & 1, kq = w >> 1, k0 = kq < 2 ? 4 * kq : 3 * kq + 2;
  u32 gprog = 0;
  for (int s = 0; s < NSEG - 1; ++s) {
    const u32 hb = OFF_HAND + s * STEP;
    const u32 need = ((u32)s >> 1) + 1;
    if (gprog < need) gprog = wait_flags<4>(ws, F_WPROG + (56 + 7 * h) * 128, need, lane, dead);
    v4u gf[4];
#pragma unroll
    for (int k = 0; k < 4; ++k) {
      const int ks = (k < 3 || kq < 2) ? k0 + k : k0;
      gf[k] = ld_dev(ws, lane * 16, OFF_GT + ((s * NH + h) * 56 + ks * 2 + tt) * 1024);
    }
    if (kq >= 2) gf[3] = (v4u){0, 0, 0, 0};
    if (s > 0) __builtin_amdgcn_s_sleep(24);
    const v4u v = poll16(ws, (w * 56 + le) * 16, hb + H_MB, dead);
    lds_w128(lds, L_MEM + (w * 56 + le) * 16, v);
    bar();
    v4f acc = {0, 0, 0, 0};
#pragma unroll
    for (int k = 0; k < 4; ++k) {
      if (k < 3 || kq < 2) acc = mfma32(gf[k], lds_r128(lds, L_MEM + (4 * (k0 + k) + g) * 128 + i8 * 16), acc);
    }
    if (hi == 0) lds_w128(lds, L_RED + ((kq * 2 + tt) * 32 + g * 8 + i8) * 16, f2u(acc));
    bar();
    if (w == 0) {
      v4f sc = u2f(lds_r128(lds, L_RED + (hi * 32 + g * 8 + i8) * 16));
#pragma unroll
      for (int k = 1; k < 4; ++k) sc += u2f(lds_r128(lds, L_RED + ((k * 2 + hi) * 32 + g * 8 + i8) * 16));
      float m = fmaxf(fmaxf(sc[0], sc[1]), fmaxf(sc[2], sc[3]));
      m = fmaxf(m, ror8f(m));
      m = xmax32(xmax16(m));
      const u32 p0 = canon(pk(ex2(sc[0] - m), ex2(sc[1] - m))), p1 = canon(pk(ex2(sc[2] - m), ex2(sc[3] - m)));
      const u32 p2 = ror8(p0), p3 = ror8(p1);
      if (hi == 0) st_dev((v4u){p0, p1, p2, p3}, ws, (h * 4 + g) * 128 + i8 * 16, hb + H_EB);
      if (lane < 8) st_dev1(canon(__float_as_uint(m)), ws, h * 128 + lane * 4, hb + H_MX);
    }
  }
}

DI void role_x(const Args& a, rsrc_t ws, int xc) {
  const int tid = threadIdx.x, lane = tid & 63, w = uni(tid >> 6), r = lane & 15, g = lane >> 4;
  bool dead = false;
  const int h = 2 * xc + (w & 1);
  u32 tprog = 0;
  for (int s = w >> 1; s < NSEG; s += 4) {
    const u32 hb = OFF_HAND + s * STEP;
    const u32 need = ((u32)s >> 1) + 1;
    if (tprog < need) tprog = wait_flags<4>(ws, F_WPROG + (56 + 7 * h + 3) * 128, need, lane, dead);
    v4u tf[2][KS];
#pragma unroll
    for (int ks = 0; ks < KS; ++ks)
#pragma unroll
      for (int tt = 0; tt < 2; ++tt) tf[tt][ks] = ld_dev(ws, lane * 16 + tt * 1024 + (ks & 1) * 2048, OFF_GT + ((s * NH + h) * 56 + 28 + (ks & ~1) * 2) * 1024);
    v4f a00 = {0, 0, 0, 0}, a01 = a00, a11 = a00, am0 = a00, am1 = a00;
#pragma unroll
    for (int kh = 0; kh < 2; ++kh) {
      v4u x0[7], x1[7];
#pragma unroll
      for (int k = 0; k < 7; ++k) {
        x0[k] = ld_pl(ws, lane * 16, OFF_XN + (s * 2 * KS + kh * 7 + k) * 1024);
        x1[k] = ld_pl(ws, lane * 16, OFF_XN + ((s * 2 + 1) * KS + kh * 7 + k) * 1024);
      }
#pragma unroll
      for (int k = 0; k < 7; ++k) {
        a00 = mfma32(x0[k], tf[0][kh * 7 + k], a00);
        a01 = mfma32(x0[k], tf[1][kh * 7 + k], a01);
        a11 = mfma32(x1[k], tf[1][kh * 7 + k], a11);
      }
    }
    if (s < NSEG - 1) {
      for (u32 it = 0;; ++it) {
        cfence();
        const u32 v = ld_dev1(ws, h * 512, hb + H_SB);
        if (uni((int)v) != -1 || dead) break;
        if (it > SPIN_CAP) { give_up(ws, dead); break; }
        __builtin_amdgcn_s_sleep(16);
      }
    }
    v4u mfr[KS];
    for (u32 it = 0;; ++it) {
      cfence();
      bool bad = false;
#pragma unroll
      for (int ks = 0; ks < KS; ++ks) mfr[ks] = ld_dev(ws, g * 128 + (r & 7) * 16 + (ks & 7) * 512, hb + H_MB + (ks >> 3) * 4096);
#pragma unroll
      for (int ks = 0; ks < KS; ++ks) bad |= unset(mfr[ks]);
      if (!any(bad) || dead) break;
      if (it > SPIN_CAP) { give_up(ws, dead); break; }
      __builtin_amdgcn_s_sleep(8);
    }
#pragma unroll
    for (int ks = 0; ks < KS; ++ks) {
      am0 = mfma32(mfr[ks], tf[0][ks], am0);
      am1 = mfma32(mfr[ks], tf[1][ks], am1);
    }
    u32 pm[2][2];
#pragma unroll
    for (int tt = 0; tt < 2; ++tt) {
      v4f lo = tt == 0 ? a00 : a01, hi = tt == 0 ? (v4f){NEGM, NEGM, NEGM, NEGM} : a11, mm = tt == 0 ? am0 : am1;
#pragma unroll
      for (int e = 0; e < 4; ++e) {
        const bool off = 4 * g + e > r;
        if (tt == 0) lo[e] = off ? NEGM : lo[e]; else hi[e] = off ? NEGM : hi[e];
      }
      float m = fmaxf(fmaxf(fmaxf(lo[0], lo[1]), fmaxf(lo[2], lo[3])), fmaxf(fmaxf(hi[0], hi[1]), fmaxf(hi[2], hi[3])));
      m = fmaxf(m, fmaxf(fmaxf(mm[0], mm[1]), fmaxf(mm[2], mm[3])));
      m = xmax32(xmax16(m));
#pragma unroll
      for (int e = 0; e < 4; ++e) { lo[e] = ex2(lo[e] - m); hi[e] = ex2(hi[e] - m); mm[e] = ex2(mm[e] - m); }
      float l = (lo[0] + lo[1]) + (lo[2] + lo[3]) + (hi[0] + hi[1]) + (hi[2] + hi[3]) + 0.5f * ((mm[0] + mm[1]) + (mm[2] + mm[3]));
      l = 1.0f / xadd32(xadd16(l));
      lo *= l; hi *= l; mm *= l;
      st_dev((v4u){pk(lo[0], lo[1]), pk(lo[2], lo[3]), pk(hi[0], hi[1]), pk(hi[2], hi[3])}, ws, lane * 16 + tt * 1024, OFF_PX + (s * NH + h) * 2048);
      pm[tt][0] = pk(mm[0], mm[1]); pm[tt][1] = pk(mm[2], mm[3]);
    }
    const auto t0 = __builtin_amdgcn_permlane16_swap(pm[0][0], pm[1][0], false, false);
    const auto t1 = __builtin_amdgcn_permlane16_swap(pm[0][1], pm[1][1], false, false);
    if (lane < 32) st_dev((v4u){t0[0], t1[0], t0[1], t1[1]}, ws, g * 1024 + ((h & 3) * 16 + r) * 16, OFF_PM + (s * 4 + (h >> 2)) * 2048);
    drain();
    if (lane == 0) __hip_atomic_fetch_add((GAS u32*)(a.ws + F_PCNT + s * 128), 1u, __ATOMIC_RELAXED, __HIP_MEMORY_SCOPE_AGENT);
  }
}

template <int MODE>
DI void ws_gemm(const Args& a, rsrc_t ws, char* lds, const v4u (&wf)[2][KS], int id, int p0, int p1) {
  const int tid = threadIdx.x, lane = tid & 63, w = uni(tid >> 6), g = lane >> 4;
  const int cg = w & 3, th = w >> 2;
  const u32 lbase = (u32)(uintptr_t)(LAS char*)lds;
  constexpr u32 XB = 56 * 1024;
  const char* xsrc = a.ws + OFF_XN + w * 7 * 1024 + lane * 16;
#pragma unroll
  for (int i = 0; i < 7; ++i) glds16(xsrc + (size_t)p0 * XB + i * 1024, lbase + (p0 & 1) * XB + (w * 7 + i) * 1024);
  drain();
  for (int p = p0; p < p1; ++p) {
    bar();
    if (p > p0 && tid == 0) {
      const int done = p == p0 + 1 ? p : p - 1;
      if (MODE < 2) { if (p != p0 + 2) st_dev1((u32)done, ws, F_WPROG + id * 128, 0); }
      else if (p != p0 + 2) __hip_atomic_fetch_add((GAS u32*)(a.ws + F_HCNT + (done - 1) * 128), 1u, __ATOMIC_RELAXED, __HIP_MEMORY_SCOPE_AGENT);
    }
    if (p + 1 < p1) {
#pragma unroll
      for (int i = 0; i < 7; ++i) glds16(xsrc + (size_t)(p + 1) * XB + i * 1024, lbase + ((p + 1) & 1) * XB + (w * 7 + i) * 1024);
    }
    v4f acc[2][2];
#pragma unroll
    for (int c = 0; c < 2; ++c)
#pragma unroll
      for (int t = 0; t < 2; ++t) acc[c][t] = (v4f){0, 0, 0, 0};
    const u32 xo = (p & 1) * XB + th * 28 * 1024 + lane * 16;
#pragma unroll
    for (int ks = 0; ks < KS; ++ks) {
      const v4u x0 = lds_r128(lds, xo + ks * 1024), x1 = lds_r128(lds, xo + (KS + ks) * 1024);
#pragma unroll
      for (int c = 0; c < 2; ++c) {
        if (MODE == 0) { acc[c][0] = mfma32(x0, wf[c][ks], acc[c][0]); acc[c][1] = mfma32(x1, wf[c][ks], acc[c][1]); }
        else { acc[c][0] = mfma32(wf[c][ks], x0, acc[c][0]); acc[c][1] = mfma32(wf[c][ks], x1, acc[c][1]); }
      }
    }
    const int s = 2 * p + th;
    if (MODE == 0) {
#pragma unroll
      for (int c = 0; c < 2; ++c)
        st_dev((v4u){pk(acc[c][0][0], acc[c][0][1]), pk(acc[c][0][2], acc[c][0][3]), pk(acc[c][1][0], acc[c][1][1]), pk(acc[c][1][2], acc[c][1][3])},
               ws, lane * 16 + c * 1024, OFF_U + ((s * 56 + id) * 8 + 2 * cg) * 1024);
    } else if (MODE == 1) {
      const int h = (id - 56) / 7, uu = ((id - 56) % 7) * 4 + cg;
#pragma unroll
      for (int t = 0; t < 2; ++t)
        st_dev((v4u){pk(acc[0][t][0], acc[0][t][1]), pk(acc[0][t][2], acc[0][t][3]), pk(acc[1][t][0], acc[1][t][1]), pk(acc[1][t][2], acc[1][t][3])},
               ws, lane * 16 + t * 1024, OFF_GT + ((s * NH + h) * 56 + uu * 2) * 1024);
    } else {
      u32 hp[2][2];
#pragma unroll
      for (int t = 0; t < 2; ++t) {
        float v[4];
#pragma unroll
        for (int e = 0; e < 4; ++e) v[e] = rbf(silu(rbf(acc[0][t][e]))) * rbf(acc[1][t][e]);
        hp[t][0] = pk(v[0], v[1]); hp[t][1] = pk(v[2], v[3]);
      }
      const auto t0 = __builtin_amdgcn_permlane16_swap(hp[0][0], hp[1][0], false, false);
      const auto t1 = __builtin_amdgcn_permlane16_swap(hp[0][1], hp[1][1], false, false);
      st_dev((v4u){t0[0], t1[0], t0[1], t1[1]}, ws, (g & 1) * 56 * 1024 + ((2 * (cg & 1) + (g >> 1)) * 16 + (lane & 15)) * 16,
             OFF_H + (s * 2 * 56 + 2 * id + (cg >> 1)) * 1024);
    }
    if (p == p0 || p + 1 == p1) drain();
    else if (MODE < 2) asm volatile("s_waitcnt vmcnt(2)" ::: "memory");
    else asm volatile("s_waitcnt vmcnt(1)" ::: "memory");
  }
  bar();
  if (tid == 0) {
    if (MODE < 2) st_dev1((u32)p1, ws, F_WPROG + id * 128, 0);
    else {
      if (p1 - p0 > 2) __hip_atomic_fetch_add((GAS u32*)(a.ws + F_HCNT + (p1 - 2) * 128), 1u, __ATOMIC_RELAXED, __HIP_MEMORY_SCOPE_AGENT);
      __hip_atomic_fetch_add((GAS u32*)(a.ws + F_HCNT + (p1 - 1) * 128), 1u, __ATOMIC_RELAXED, __HIP_MEMORY_SCOPE_AGENT);
    }
  }
}

DI void worker_front(const Args& a, rsrc_t ws, int id, char* lds) {
  const int tid = threadIdx.x, lane = tid & 63, w = uni(tid >> 6);
  const int cg = w & 3;
  v4u wf[2][KS];
#pragma unroll
  for (int c = 0; c < 2; ++c)
#pragma unroll
    for (int ks = 0; ks < KS; ++ks) wf[c][ks] = ld_pl(ws, lane * 16 + (ks & 3) * 1024, OFF_WB + id * WBSZ + ((2 * cg + c) * KS + (ks & ~3)) * 1024);
  if (id < 56) ws_gemm<0>(a, ws, lds, wf, id, 0, 32);
  else ws_gemm<1>(a, ws, lds, wf, id, 0, 32);
  const int fb = id / 6, r6 = id % 6;
  stage64(a.w1 + fb * 64, a.w3 + fb * 64, FF, lds, tid);
  bar();
#pragma unroll
  for (int c = 0; c < 2; ++c)
#pragma unroll
    for (int ks = 0; ks < KS; ++ks) wf[c][ks] = tr_frag(lds, c * SIMG, SP, ks, lane, cg * 32 + (lane & 3) * 8);
  bar();
  const int p0 = r6 < 2 ? 6 * r6 : 5 * r6 + 2, p1 = r6 < 2 ? p0 + 6 : p0 + 5;
  ws_gemm<2>(a, ws, lds, wf, fb, p0, p1);
}

DI void worker_back(const Args& a, rsrc_t ws, int id, char* lds) {
  const int tid = threadIdx.x, lane = tid & 63, w = uni(tid >> 6), r = lane & 15, g = lane >> 4;
  bool dead = false;
  const int cb = id % 14, r12 = id / 14;
  v4u w2f[2][7];
  {
    constexpr u32 P2 = 80;
    v4u t[14];
#pragma unroll
    for (int i = 0; i < 14; ++i) t[i] = *(const GAS v4u*)(a.w2 + (size_t)(w * 224 + i * 16 + (lane >> 2)) * DM + cb * 32 + (lane & 3) * 8);
#pragma unroll
    for (int i = 0; i < 14; ++i) lds_w128(lds, (w * 224 + i * 16 + (lane >> 2)) * P2 + (lane & 3) * 16, t[i]);
    asm volatile("s_waitcnt lgkmcnt(0)" ::: "memory");
#pragma unroll
    for (int c = 0; c < 2; ++c)
#pragma unroll
      for (int k = 0; k < 7; ++k) w2f[c][k] = tr_frag(lds, w * 224 * P2, P2, k, lane, (lane & 3) * 16 + c * 8);
  }
  if (w == 0) wait_flags<4>(ws, F_WPROG + 4 * cb * 128, 32, lane, dead);
  const u32 vo_u = (r >> 2) * 8192 + ((r & 3) + 16 * g) * 16;
  const u32 vo_z = (r >> 2) * 2048 + (g * 8 + (r & 3)) * 16;
  const int ot = w & 1, oc = (w >> 1) & 1, orp = w >> 2;
  for (int s = r12; s < NSEG; s += 12) {
    if (w == 0) wait_flags<1>(ws, F_HCNT + (s >> 1) * 128, 28, lane, dead);
    bar();
    v4u hf[2][7], uf[2][2];
#pragma unroll
    for (int t = 0; t < 2; ++t)
#pragma unroll
      for (int k = 0; k < 7; ++k) hf[t][k] = ld_dev(ws, lane * 16 + (k & 3) * 1024, OFF_H + ((s * 2 + t) * 56 + w * 7 + (k & 4)) * 1024);
#pragma unroll
    for (int hh = 0; hh < 2; ++hh)
#pragma unroll
      for (int c = 0; c < 2; ++c) uf[hh][c] = ld_dev(ws, vo_u + (hh * 8 + 4 * c) * 16, OFF_U + ((s * 56 + 4 * cb) * 8 + w) * 1024);
    const size_t oidx = (size_t)(s * 32 + ot * 16 + r) * DM + cb * 32 + g * 8 + oc * 4 + orp * 2;
    const u32 xin = *(const GAS u32*)(a.x + oidx);
    v4f ag[2][2], as[2][2];
#pragma unroll
    for (int c = 0; c < 2; ++c)
#pragma unroll
      for (int t = 0; t < 2; ++t) { ag[c][t] = (v4f){0, 0, 0, 0}; as[c][t] = (v4f){0, 0, 0, 0}; }
#pragma unroll
    for (int k = 0; k < 7; ++k)
#pragma unroll
      for (int c = 0; c < 2; ++c)
#pragma unroll
        for (int t = 0; t < 2; ++t) ag[c][t] = mfma32(w2f[c][k], hf[t][k], ag[c][t]);
#pragma unroll
    for (int c = 0; c < 2; ++c)
#pragma unroll
      for (int t = 0; t < 2; ++t) lds_w128(lds, ((w * 8 + c * 2 + t) * 64 + lane) * 16, f2u(ag[c][t]));
    if (w == 0) {
      wait_flags<1>(ws, F_PCNT + s * 128, 16, lane, dead);
      wait_flags<4>(ws, F_ZPROG + 4 * cb * 128, (u32)s + 1, lane, dead);
    }
    bar();
    v4u pf[2][2], zf[2], pmf;
#pragma unroll
    for (int hh = 0; hh < 2; ++hh)
#pragma unroll
      for (int t = 0; t < 2; ++t) pf[hh][t] = ld_dev(ws, lane * 16 + (hh * 2 + t) * 1024, OFF_PX + (s * NH + 2 * w) * 2048);
#pragma unroll
    for (int c = 0; c < 2; ++c) zf[c] = ld_dev(ws, vo_z + c * 64, OFF_Z + (s * 56 + 4 * cb) * 2048 + (w >> 1) * 512);
    pmf = ld_dev(ws, lane * 16, OFF_PM + (s * 8 + w) * 1024);
    v2f gs = {0, 0};
#pragma unroll
    for (int v = 0; v < 8; ++v) gs += __builtin_bit_cast(v2f, lds_r64(lds, ((v * 8 + oc * 2 + ot) * 64 + lane) * 16 + orp * 8));
    const float g0 = rbf(silu(rbf(gs[0]))), g1 = rbf(silu(rbf(gs[1])));
#pragma unroll
    for (int hh = 0; hh < 2; ++hh)
#pragma unroll
      for (int c = 0; c < 2; ++c)
#pragma unroll
        for (int t = 0; t < 2; ++t) as[c][t] = mfma32(uf[hh][c], pf[hh][t], as[c][t]);
#pragma unroll
    for (int c = 0; c < 2; ++c) {
      if (w & 1) as[c][1] = mfma32(zf[c], pmf, as[c][1]);
      else as[c][0] = mfma32(zf[c], pmf, as[c][0]);
    }
#pragma unroll
    for (int c = 0; c < 2; ++c)
#pragma unroll
      for (int t = 0; t < 2; ++t) lds_w128(lds, ((w * 8 + 4 + c * 2 + t) * 64 + lane) * 16, f2u(as[c][t]));
    bar();
    v2f ss = {0, 0};
#pragma unroll
    for (int v = 0; v < 8; ++v) ss += __builtin_bit_cast(v2f, lds_r64(lds, ((v * 8 + 4 + oc * 2 + ot) * 64 + lane) * 16 + orp * 8));
    *(GAS u32*)(a.out + oidx) = pk(bflo(xin) + rbf(rbf(ss[0]) * g0), bfhi(xin) + rbf(rbf(ss[1]) * g1));
  }
}

__global__ void __launch_bounds__(512, 2) k_main(Args a) {
  extern __shared__ __attribute__((aligned(16))) char lds[];
  const rsrc_t ws = __builtin_amdgcn_make_buffer_rsrc(a.ws, 0, WS_END, 0x00020000);
  const int b = blockIdx.x, lab = b & 7, slot = b >> 3;
  if (lab < 2) { if (a.mode & 2) role_chain(a, ws, slot * 2 + lab, lds); }
  else if (lab == 2 && slot < 16) { if (a.mode & 2) role_g(a, ws, slot, lds); }
  else if (lab == 2 && slot < 24) { if (a.mode & 2) role_x(a, ws, slot - 16); }
  else {
    const int id = lab == 2 ? slot - 24 : 8 + (lab - 3) * 32 + slot;
    if (a.mode & 1) worker_front(a, ws, id, lds);
    if (a.mode & 4) { bar(); worker_back(a, ws, id, lds); }
  }
}

}

extern "C" void kernel_launch(void* const* d_in, const int* in_sizes, int n_in, void* d_out, int out_size, void* d_ws,
                              size_t ws_size, hipStream_t stream) {
  Args a;
  a.x = (const u16*)d_in[0]; a.mem0 = (const u16*)d_in[1]; a.wq = (const u16*)d_in[2]; a.wk = (const u16*)d_in[3];
  a.wv = (const u16*)d_in[4]; a.wo = (const u16*)d_in[5]; a.w1 = (const u16*)d_in[8]; a.w3 = (const u16*)d_in[9];
  a.w2 = (const u16*)d_in[10]; a.out = (u16*)d_out;
  a.ws = (char*)(((uintptr_t)d_ws + ((2u << 20) - 1)) & ~(uintptr_t)((2u << 20) - 1));
  a.mode = 7;
  (void)hipFuncSetAttribute((const void*)k_prep, hipFuncAttributeMaxDynamicSharedMemorySize, LDS_PREP);
  (void)hipFuncSetAttribute((const void*)k_main, hipFuncAttributeMaxDynamicSharedMemorySize, LDS_MAIN);
  k_prep<<<256, 512, LDS_PREP, stream>>>(a);
  k_main<<<256, 512, LDS_MAIN, stream>>>(a);
}
```

```cpp
#include <hip/hip_runtime.h>
#include <hip/hip_bf16.h>
#include <stdint.h>
#include <type_traits>

namespace {

typedef unsigned int u32;
typedef unsigned short u16;
typedef unsigned long long u64;
typedef u32 v8u __attribute__((ext_vector_type(8)));
typedef u32 v4u __attribute__((ext_vector_type(4)));
typedef u32 v2u __attribute__((ext_vector_type(2)));
typedef float v4f __attribute__((ext_vector_type(4)));
typedef float v2f __attribute__((ext_vector_type(2)));
typedef short v4s __attribute__((ext_vector_type(4)));
typedef unsigned short v2h __attribute__((ext_vector_type(2)));
typedef __bf16 v16bf __attribute__((ext_vector_type(16)));
typedef __bf16 v8bf __attribute__((ext_vector_type(8)));
typedef __bf16 v2bf __attribute__((ext_vector_type(2)));
#define LAS __attribute__((address_space(3)))
#define GAS __attribute__((address_space(1)))
#define DI __device__ __forceinline__
typedef __amdgpu_buffer_rsrc_t rsrc_t;

constexpr int NSEG = 64, DM = 448, NH = 16, FF = 1792, KS = 14;
constexpr float SCL = 1.4426950408889634f / 16.0f;
constexpr float NEGM = -1.0e9f * 1.4426950408889634f;

constexpr u32 STEP = 131072;
constexpr u32 H_S = 32768;
constexpr u32 OFF_FLAG = 8u << 20;
constexpr u32 F_WPROG = OFF_FLAG, F_ZPROG = F_WPROG + 112 * 128, F_HCNT = F_ZPROG + 64 * 128, F_PCNT = F_HCNT + 32 * 128,
              F_TAB = F_PCNT + 64 * 128, F_TMO = F_TAB + 64 * 128, F_END = F_TMO + 128;
constexpr u32 OFF_XN = 9u << 20;
constexpr u32 OFF_WB = OFF_XN + 128 * KS * 1024;
constexpr u32 WBSZ = 8 * KS * 1024;
constexpr u32 OFF_G = OFF_WB + 112 * WBSZ;
constexpr u32 OFF_U = OFF_G + NSEG * NH * 28 * 1024;
constexpr u32 OFF_H = OFF_U + NSEG * 56 * 8192;
constexpr u32 OFF_PX = OFF_H + 128 * 56 * 1024;
constexpr u32 OFF_PM = OFF_PX + NSEG * NH * 2048;
constexpr u32 OFF_Z = OFF_PM + NSEG * 4 * 2048;
constexpr u32 OFF_GM = OFF_Z + NSEG * 56 * 2048;
constexpr u32 WS_END = OFF_GM + NSEG * NH * 7168;
constexpr u32 OOB = 0x40000000u;

constexpr int LDS_MAIN = 146 * 1024;
constexpr int LDS_PREP = 120 * 1024;
constexpr u32 SPIN_CAP = 1u << 18;

struct Args {
  const u16* x; const u16* mem0; const u16* wq; const u16* wk; const u16* wv; const u16* wo;
  const u16* w1; const u16* w3; const u16* w2; u16* out; char* ws;
};

DI v4f mfma32(v4u a, v4u b, v4f c) {
  return __builtin_amdgcn_mfma_f32_16x16x32_bf16(__builtin_bit_cast(v8bf, a), __builtin_bit_cast(v8bf, b), c, 0, 0, 0);
}
DI v4f mfma16(v2u a, v2u b, v4f c) {
  return __builtin_amdgcn_mfma_f32_16x16x16bf16_1k(__builtin_bit_cast(v4s, a), __builtin_bit_cast(v4s, b), c, 0, 0, 0);
}
DI v4f smf(v4u a, v8u b, v4f c, int idx) {
  return __builtin_amdgcn_smfmac_f32_16x16x64_bf16(__builtin_bit_cast(v8bf, a), __builtin_bit_cast(v16bf, b), c, idx, 0, 0);
}
DI v8u cat(v4u a, v4u b) { return __builtin_shufflevector(a, b, 0, 1, 2, 3, 4, 5, 6, 7); }
DI u32 pk(float lo, float hi) { v2f t = {lo, hi}; v2bf b = __builtin_convertvector(t, v2bf); return __builtin_bit_cast(u32, b); }
DI float rbf(float x) { return __uint_as_float(pk(x, 0.f) << 16); }
DI float bflo(u32 v) { return __uint_as_float(v << 16); }
DI float bfhi(u32 v) { return __uint_as_float(v & 0xffff0000u); }
DI u32 canon(u32 v) { return v < 0xfffffffeu ? v : 0xfffffffeu; }
DI float ex2(float x) { return __builtin_amdgcn_exp2f(x); }
DI float rcp(float x) { return __builtin_amdgcn_rcpf(x); }
DI float silu(float x) { return x / (1.0f + __expf(-x)); }
DI int uni(int v) { return __builtin_amdgcn_readfirstlane(v); }
DI void bar() { asm volatile("s_waitcnt lgkmcnt(0)\n\ts_barrier" ::: "memory"); }
DI void drain() { asm volatile("s_waitcnt vmcnt(0)" ::: "memory"); }
DI void cfence() { asm volatile("" ::: "memory"); }
DI void fence() { __builtin_amdgcn_sched_barrier(0); }
DI v4u ld_dev(rsrc_t r, u32 v, u32 s) { return __builtin_amdgcn_raw_buffer_load_b128(r, v, s, 16); }
DI u32 ld_dev1(rsrc_t r, u32 v, u32 s) { return __builtin_amdgcn_raw_buffer_load_b32(r, v, s, 16); }
DI v4u ld_pl(rsrc_t r, u32 v, u32 s) { return __builtin_amdgcn_raw_buffer_load_b128(r, v, s, 0); }
DI void st_dev(v4u d, rsrc_t r, u32 v, u32 s) { asm("" : "+v"(d)); __builtin_amdgcn_raw_buffer_store_b128(d, r, v, s, 16); asm volatile("s_nop 1" :: "v"(d) : "memory"); }
DI void st_dev1(u32 d, rsrc_t r, u32 v, u32 s) { __builtin_amdgcn_raw_buffer_store_b32(d, r, v, s, 16); }
DI void st_devh(u32 d, rsrc_t r, u32 v, u32 s) { __builtin_amdgcn_raw_buffer_store_b16((u16)d, r, v, s, 16); }
DI void st_pl(v4u d, rsrc_t r, u32 v, u32 s) { __builtin_amdgcn_raw_buffer_store_b128(d, r, v + s, 0, 16); }
DI v4u lds_r128(char* l, u32 o) { return *(LAS v4u*)(l + o); }
DI v2u lds_r64(char* l, u32 o) { return *(LAS v2u*)(l + o); }
DI u32 lds_r32(char* l, u32 o) { return *(LAS u32*)(l + o); }
DI void lds_w128(char* l, u32 o, v4u v) { *(LAS v4u*)(l + o) = v; }
DI void lds_w64(char* l, u32 o, v2u v) { *(LAS v2u*)(l + o) = v; }
DI void lds_w32(char* l, u32 o, u32 v) { *(LAS u32*)(l + o) = v; }
DI void lds_w16(char* l, u32 o, u16 v) { *(LAS u16*)(l + o) = v; }
DI v2u lds_tr(char* l, u32 o) { return __builtin_bit_cast(v2u, __builtin_amdgcn_ds_read_tr16_b64_v4i16((LAS v4s*)(l + o))); }
DI v4u f2u(v4f v) { return __builtin_bit_cast(v4u, v); }
DI v4f u2f(v4u v) { return __builtin_bit_cast(v4f, v); }
DI float xmax16(float v) { u32 u = __float_as_uint(v); auto s = __builtin_amdgcn_permlane16_swap(u, u, false, false); return fmaxf(__uint_as_float(s[0]), __uint_as_float(s[1])); }
DI float xmax32(float v) { u32 u = __float_as_uint(v); auto s = __builtin_amdgcn_permlane32_swap(u, u, false, false); return fmaxf(__uint_as_float(s[0]), __uint_as_float(s[1])); }
DI float xadd16(float v) { u32 u = __float_as_uint(v); auto s = __builtin_amdgcn_permlane16_swap(u, u, false, false); return __uint_as_float(s[0]) + __uint_as_float(s[1]); }
DI float xadd32(float v) { u32 u = __float_as_uint(v); auto s = __builtin_amdgcn_permlane32_swap(u, u, false, false); return __uint_as_float(s[0]) + __uint_as_float(s[1]); }
template <int CTRL> DI u32 dpp(u32 v) { return (u32)__builtin_amdgcn_update_dpp(0, (int)v, CTRL, 0xf, 0xf, true); }
template <int CTRL> DI float dppf(float v) { return __uint_as_float(dpp<CTRL>(__float_as_uint(v))); }
constexpr int ROR8 = 0x128, QX1 = 0xB1, QX2 = 0x4E, HMIR = 0x141, Q2323 = 0xEE;
DI float wsum(float v) { v += dppf<QX1>(v); v += dppf<QX2>(v); v += dppf<HMIR>(v); v += dppf<ROR8>(v); return xadd32(xadd16(v)); }
DI bool unset(v4u v) { u32 m = v.x > v.y ? v.x : v.y; m = m > v.z ? m : v.z; m = m > v.w ? m : v.w; return m == 0xffffffffu; }
DI bool any(bool p) { return __builtin_amdgcn_ballot_w64(p) != 0ull; }
DI u32 subs(u32 a, u32 b) { return __builtin_bit_cast(u32, __builtin_elementwise_sub_sat(__builtin_bit_cast(v2h, a), __builtin_bit_cast(v2h, b))); }
DI void glds16(const void* gsrc, u32 lds_dst) {
  u32 keep;
  asm volatile("s_mov_b32 %0, m0\n\ts_mov_b32 m0, %2\n\ts_nop 0\n\tglobal_load_lds_dwordx4 %1, off\n\ts_mov_b32 m0, %0"
               : "=&s"(keep) : "v"(gsrc), "s"(lds_dst) : "memory");
}
DI void give_up(rsrc_t ws, bool& dead) { dead = true; st_dev1(1u, ws, F_TMO, 0); }

template <int NL> DI u32 lanemin(u32 v) {
  u32 mn = (u32)__builtin_amdgcn_readlane((int)v, 0);
#pragma unroll
  for (int l = 1; l < NL; ++l) { const u32 t = (u32)__builtin_amdgcn_readlane((int)v, l); asm("s_min_u32 %0, %1, %2" : "=s"(mn) : "s"(mn), "s"(t) : "scc"); }
  return mn;
}
template <int NL, bool FAR>
DI u32 wait_at(rsrc_t ws, u32 vo, u32 base, u32 want, bool& dead) {
  for (u32 it = 0;; ++it) {
    cfence();
    const u32 mn = lanemin<NL>(ld_dev1(ws, vo, base));
    if (mn >= want) return mn;
    if (dead) return want;
    if (it > SPIN_CAP) { give_up(ws, dead); return want; }
    if (FAR) for (u32 i = mn + 2; i < want; ++i, it += 8) __builtin_amdgcn_s_sleep(44);
    __builtin_amdgcn_s_sleep(FAR ? 2 : 4);
  }
}
template <int NL, bool FAR = false>
DI u32 wait_flags(rsrc_t ws, u32 base, u32 want, int lane, bool& dead) { return wait_at<NL, FAR>(ws, (u32)(lane < NL ? lane : 0) * 128u, base, want, dead); }
DI v4u poll16(rsrc_t ws, u32 vo, u32 so, bool& dead) {
  for (u32 it = 0;; ++it) {
    cfence();
    const v4u v = ld_dev(ws, vo, so);
    if (!any(unset(v)) || dead) return v;
    if (it > SPIN_CAP) { give_up(ws, dead); return v; }
  }
}


constexpr u32 SP = 144, SIMG = DM * SP;
DI void stage64(const u16* p0, const u16* p1, int rstride, char* lds, int tid) {
  v4u t[14];
#pragma unroll
  for (int it = 0; it < 14; ++it) {
    const int row = (it % 7) * 64 + (tid >> 3);
    t[it] = *(const GAS v4u*)((it < 7 ? p0 : p1) + (size_t)row * rstride + (tid & 7) * 8);
  }
#pragma unroll
  for (int it = 0; it < 14; ++it) {
    const int row = (it % 7) * 64 + (tid >> 3);
    lds_w128(lds, (it < 7 ? 0u : SIMG) + row * SP + (tid & 7) * 16, t[it]);
  }
}
DI v4u tr_frag(char* lds, u32 base, u32 pitch, int ks, int lane, u32 piece) {
  const u32 g = lane >> 4, q4 = (lane & 15) >> 2;
  const u32 o = base + (32 * ks + 8 * g + q4) * pitch + piece;
  const v2u a = lds_tr(lds, o), b = lds_tr(lds, o + 4 * pitch);
  return (v4u){a.x, a.y, b.x, b.y};
}

__global__ void __launch_bounds__(512, 2) k_prep(Args a) {
  extern __shared__ __attribute__((aligned(16))) char lds[];
  const int tid = threadIdx.x, lane = tid & 63, w = uni(tid >> 6), r = lane & 15, g = lane >> 4;
  const rsrc_t ws = __builtin_amdgcn_make_buffer_rsrc(a.ws, 0, WS_END, 0x00020000);
  const int b = blockIdx.x;
  const int gi = (b & 7) * 4 + ((b >> 3) & 3), ty = gi >> 4, h = gi & 15, qd = (b >> 5) & 3, ks = 7 * (b >> 7) + (w < 7 ? w : 6);
  constexpr u32 RAW = 56 * 1024, RP = 240;
  const int le = lane < 56 ? lane : 55, lr = le / 14, lc = le % 14;
  const u16* np = ty == 0 ? a.wq + h * 256 + w * 32 : a.wo + (size_t)(h * 256 + w * 32) * DM + qd * 112;
  const u32 nv = ty == 0 ? (16 * (r >> 3) + 2 * ((r >> 2) & 1) + (r & 1) + 4 * ((r >> 1) & 1)) * 4096 + g * 8 : lr * DM + lc * 8;
  v4u t[8];
#pragma unroll
  for (int i = 0; i < 8; ++i) {
    const int ct = 7 * qd + (i < 7 ? i : 6);
    t[i] = *(const GAS v4u*)(np + (u32)uni(ty == 0 ? (32 * (ct >> 1) + 8 * (ct & 1)) * 4096 : 4 * i * DM) + nv);
  }
  const v4u xv = *(const GAS v4u*)(a.x + (size_t)(b * 8 + w) * DM + le * 8);
  fence();
  const u16* kp = (ty == 0 ? a.wk : a.wv) + (size_t)(32 * ks + 8 * (r >> 2) + (r & 3)) * 4096 + h * 256 + g * 8;
  v4u kf[8][2];
#pragma unroll
  for (int e = 0; e < 8; ++e) {
    kf[e][0] = *(const GAS v4u*)(kp + e * 32);
    kf[e][1] = *(const GAS v4u*)(kp + 4 * 4096 + e * 32);
  }
  fence();
  if (b == 1) for (u32 o = tid * 16; o < F_END - OFF_FLAG; o += 8192) st_pl((v4u){0, 0, 0, 0}, ws, OFF_FLAG + o, 0);
  if (ty) {
#pragma unroll
    for (int i = 0; i < 8; ++i) {
      const u32 o = RAW + (w * 32 + 4 * i + lr) * RP + (lc >> 1) * 32 + (lc & 1) * 8;
      lds_w64(lds, o, (v2u){t[i].x, t[i].z});
      lds_w64(lds, o + 16, (v2u){t[i].y, t[i].w});
    }
    asm volatile("s_waitcnt lgkmcnt(0)" ::: "memory");
#pragma unroll
    for (int i = 0; i < 7; ++i) t[i] = tr_frag(lds, RAW, RP, w, lane, i * 32 + (lane & 3) * 8);
  }
#pragma unroll
  for (int i = 0; i < 7; ++i) lds_w128(lds, (i * 8 + w) * 1024 + lane * 16, t[i]);
  bar();
  auto norm = [&]() {
    float f[8] = {bflo(xv.x), bfhi(xv.x), bflo(xv.y), bfhi(xv.y), bflo(xv.z), bfhi(xv.z), bflo(xv.w), bfhi(xv.w)};
    float sum = 0.f;
#pragma unroll
    for (int k = 0; k < 8; ++k) sum += f[k];
    const float mean = wsum(lane < 56 ? sum : 0.f) * (1.0f / DM);
    float sq = 0.f;
#pragma unroll
    for (int k = 0; k < 8; ++k) sq += (f[k] - mean) * (f[k] - mean);
    sq = wsum(lane < 56 ? sq : 0.f);
    const float mb = rbf(mean), den = 1.0f / rbf(sqrtf(rbf(rbf(sq * (1.0f / DM)) + 1e-5f)));
#pragma unroll
    for (int k = 0; k < 8; ++k) f[k] = rbf(f[k] - mb) * den;
    const v4u o = {pk(f[0], f[1]), pk(f[2], f[3]), pk(f[4], f[5]), pk(f[6], f[7])};
    st_pl(o, ws, lane < 56 ? OFF_XN + ((b >> 1) * KS + (le >> 2)) * 1024 + ((le & 3) * 16 + (b & 1) * 8 + w) * 16 : OOB, 0);
  };
  if (w >= 4) norm();
  if (w == 7) {
#pragma unroll
    for (u32 i = 0; i < 2; ++i) {
      const u32 o = (b * 76 + i * 64 + lane) * 16;
      const u32 d = o < 114688 ? OFF_GM + 63 * NH * 7168 + o : o < 229376 ? OFF_Z + 63 * 56 * 2048 + o - 114688 : o < 294912 ? OFF_PX + 62 * NH * 2048 + o - 229376 : OFF_PM + 62 * 8192 + o - 294912;
      st_pl((v4u){~0u, ~0u, ~0u, ~0u}, ws, i * 64 + lane < 76 ? d : OOB, 0);
    }
    if (b < 32) {
      const u32 c = (b & 7) * 64 + lane, ln = c >> 3;
      v4u m0 = {~0u, ~0u, ~0u, ~0u};
      if (c < 448) {
        const GAS u32* p = (const GAS u32*)(a.mem0 + (c & 7) * DM + 16 * (ln >> 1) + 2 * (ln & 1));
        m0 = (v4u){canon(p[0]), canon(p[2]), canon(p[4]), canon(p[6])};
      }
      st_pl(m0, ws, (b >> 3) * 8192 + c * 16, 0);
    }
    return;
  }
  v4f acc[2][7];
#pragma unroll
  for (int i = 0; i < 7; ++i) { acc[0][i] = (v4f){0, 0, 0, 0}; acc[1][i] = acc[0][i]; }
#pragma unroll
  for (int e = 0; e < 8; ++e)
#pragma unroll
    for (int i = 0; i < 7; ++i) {
      const v4u nf = lds_r128(lds, (i * 8 + e) * 1024 + lane * 16);
      acc[0][i] = mfma32(kf[e][0], nf, acc[0][i]);
      acc[1][i] = mfma32(kf[e][1], nf, acc[1][i]);
    }
  const float sc = ty == 0 ? SCL : 1.0f;
#pragma unroll
  for (int i = 0; i < 7; ++i) {
    const int ct28 = 7 * qd + i;
    u32 dst;
    if (ty == 0) dst = 56 * WBSZ + (28 * h + ct28) * KS * 1024 + lane * 16;
    else dst = (2 * ct28 + (r >> 3)) * WBSZ + (h >> 1) * KS * 1024 + (h & 1) * 512 + g * 128 + (r & 7) * 16;
    const v4f lo = acc[0][i] * sc, hi = acc[1][i] * sc;
    const v4u o = {pk(lo[0], lo[1]), pk(lo[2], lo[3]), pk(hi[0], hi[1]), pk(hi[2], hi[3])};
    st_pl(o, ws, OFF_WB + dst + ks * 1024, 0);
  }
  if (w < 4) norm();
}

constexpr u32 L_MEM = 0, L_IMG = 7168, L_RED = 9216, L_ZA = 10240, L_STAMP = 16384, L_RING = 32768;
#define CLOB "v24", "v25", "v26", "v27", "v28", "v29", "v30", "v31", "v32", "v33", "v34", "v35", "v36", "v37", "v38", "v39", "v40", "v41", "v42", "v43", "v44", "v45", "v46", "v47", "v48", "v49", "v50", "v51", "v52", "v53", "v54", "v55", "v56", "v57", "v58", "v59", "v60", "v61", "v62", "v63", "v64", "v65", "v66", "v67", "v68", "v69", "v70", "v71", "v72", "v73", "v74", "v75", "v76", "v77", "v78", "v79", "v80", "v81", "v82", "v83", "v84", "v85", "v86", "v87", "v88", "v89", "v90", "v91", "v92", "v93", "v94", "v95", "v96", "v97", "v98", "v99", "v100", "v101", "v102", "v103", "v104", "v105", "v106", "v107", "v108", "v109", "v110", "v111", "v112", "v113", "v114", "v115", "v116", "v117", "v118", "v119", "v120", "v121", "v122", "v123", "v124", "v125", "v126", "v127", "v128", "v129", "v130", "v131", "v132", "v133", "v134", "v135", "v136", "v137", "v138", "v139", "v140", "v141", "v142", "v143", "v144", "v145", "v146", "v147", "v148", "v149", "v150", "v151", "v152", "v153", "v154", "v155", "v156", "v157", "v158", "v159", "v160", "v161", "v162", "v163", "v164", "v165", "v166", "v167", "v168", "v169", "v170", "v171", "v172", "v173", "v174", "v175", "v176", "v177", "v178", "v179", "v180", "v181", "v182", "v183", "v184", "v185", "v186", "v187", "v188", "v189", "v190", "v191", "v192", "v193", "v194", "v195", "v196", "v197", "v198", "v199", "v200", "v201", "v202", "v203", "v204", "v205", "v206", "v207", "v208", "v209", "v210", "v211", "v212", "v213", "v214", "v215", "v216", "v217", "v218", "v219", "v220", "v221", "v222", "v223", "v224", "v225", "v226", "v227", "v228", "v229", "v230", "v231", "v232", "v233", "v234", "v235", "v236", "v237", "v238", "v239", "s36", "s37", "s38", "s39", "s40", "s41", "s42", "s43", "s44", "s45", "s46", "s47", "s48", "s49", "s50", "s51", "s52", "s53", "s54", "s55", "s56", "s57", "s58", "s59", "s60", "s61", "s62", "s63", "s64", "s65", "s66", "s67", "s68", "s69", "s70", "s71", "s72", "s73", "s74", "s75", "s76", "s77", "s78", "s79", "s80", "s81", "s82", "s83", "s84", "s85", "s86", "s87", "s88", "s89", "s90", "s91", "s92", "s93", "s94", "s95", "s96", "s97", "s98", "s99", "vcc", "scc", "memory"
#define CLOBH "v40", "v41", "v42", "v43", "v44", "v45", "v46", "v47", "v48", "v49", "v50", "v51", "v52", "v53", "v54", "v55", "v56", "v57", "v58", "v59", "v60", "v61", "v62", "v63", "v64", "v65", "v66", "v67", "v68", "v69", "v70", "v71", "v72", "v73", "v74", "v75", "v76", "v77", "v78", "v79", "v80", "v81", "v82", "v83", "v84", "v85", "v86", "v87", "v88", "v89", "v90", "v91", "v92", "v93", "v94", "v95", "v96", "v97", "v98", "v99", "v100", "v101", "v102", "v103", "v104", "v105", "v106", "v107", "v108", "v109", "v110", "v111", "v112", "v113", "v114", "v115", "v116", "v117", "v118", "v119", "v120", "v121", "v122", "v123", "v124", "v125", "v126", "v127", "v128", "v129", "v130", "v131", "v132", "v133", "v134", "v135", "v136", "v137", "v138", "v139", "v140", "v141", "v142", "v143", "v144", "v145", "v146", "v147", "v148", "v149", "v150", "v151", "v152", "v153", "v154", "v155", "v156", "v157", "v158", "v159", "v160", "v161", "v162", "v163", "v164", "v165", "v166", "v167", "v168", "v169", "v170", "v171", "v172", "v173", "v174", "v175", "v176", "v177", "v178", "v179", "v180", "v181", "v182", "v183", "v184", "v185", "v186", "v187", "v188", "v189", "v190", "v191", "v192", "v193", "v194", "v195", "v196", "v197", "v198", "v199", "v200", "v201", "v202", "v203", "v204", "v205", "v206", "v207", "v208", "v209", "v210", "v211", "v212", "v213", "v214", "v215", "v216", "v217", "v218", "v219", "v220", "v221", "v222", "v223", "v224", "v225", "v226", "v227", "v228", "v229", "v230", "v231", "v232", "v233", "v234", "v235", "v236", "v237", "v238", "v239", "v240", "v241", "v242", "v243", "v244", "v245", "v246", "v247", "v248", "v249", "v250", "v251", "v252", "v253", "v254", "v255", "s36", "s37", "s38", "s39", "s40", "s41", "s42", "s43", "s44", "s45", "s46", "s47", "s48", "s49", "s50", "s51", "s52", "s53", "s54", "s55", "s56", "s57", "s58", "s59", "s60", "s61", "s62", "s63", "s64", "s65", "s66", "s67", "s68", "s69", "s70", "s71", "s72", "s73", "s74", "s75", "s76", "s77", "s78", "s79", "s80", "s81", "s82", "s83", "s84", "s85", "s86", "s87", "s88", "s89", "s90", "s91", "s92", "s93", "s94", "s95", "s96", "s97", "s98", "s99", "vcc", "scc", "memory"
DI v4u score_sel(u32 r) {
  const u32 s1 = 2 * ((r >> 2) ^ ((r >> 1) & 1)) + (r & 1);
  v4u sel;
#pragma unroll
  for (u32 d = 0; d < 4; ++d) sel[d] = (s1 == 2 * d ? 0x3f80u : 0u) | (s1 == 2 * d + 1 ? 0x3f800000u : 0u);
  return sel;
}
template <int K>
DI void chain_main(const Args& a, u32 xb, u32 slot, u32 lb) {
  const u32 lane = threadIdx.x & 63, r = lane & 15, g = lane >> 4;
  const u32 h = 8 * xb + (slot >> 2), j = slot & 3, q = 32 * xb + slot;
  const u32 half = (r >> 1) & 1, rm = 2 * (r >> 2) + (r & 1), hh = r >> 3, i8 = r & 7;
  const u32 kR = 4 - 4 * xb, kL = 4 * xb;
  const u32 fo = (2 * g + half) * 128 + rm * 16;
  constexpr u32 tA = K == 0 ? 0 : K == 1 ? 1 : K == 2 ? 4 : 5;
  auto wofs = [&](u32 t) { const u32 n = 112 * j + 16 * t + r; return OFF_WB + 56 * WBSZ + (28 * h + 2 * (n >> 5) + ((n >> 2) & 1)) * KS * 1024 + (g * 16 + 4 * ((n & 31) >> 3) + (n & 3)) * 16; };
  auto kbof = [&](u32 sl) { return sl < 3 ? kR + sl : sl < 6 ? kL + sl - 3 : 3u; };
  const u32 vg = OFF_G + h * 28 * 1024 + ((g >> 1) * 2 + (i8 >> 2)) * 1024 + ((2 * (g & 1) + hh) * 16 + 4 * j + (r & 3)) * 16;
  const v4u sel = score_sel(r);
  const v4u rs = {(u32)(uintptr_t)a.ws, (u32)((uintptr_t)a.ws >> 32), WS_END, 0x00020000u};
  u32 cap = SPIN_CAP;
#define MAIN_OPS : [cap] "+&s"(cap) \
  : [rs] "s"(rs), [oR] "s"(kR * 2048), [oL] "s"(kL * 2048), [oM] "s"(3 * 2048), [lb] "s"(lb), \
    [vm0] "v"((kL + (K < 3 ? K : 0)) * 1024 + fo), [vm1] "v"((K < 3 ? kR + K : 3) * 1024 + fo), [lf] "v"(lb + fo), [ll] "v"(lb + lane * 16), \
    [limg] "v"(lb + L_IMG + (K >> 1) * 1024 + (16 * (K & 1) + r) * 32 + g * 8), [lred] "v"(lb + L_RED + (K * 8 + i8) * 16 + g * 4), \
    [vl] "v"(((2 * (kL + K) + hh) * 4 + g) * 128 + i8 * 16 + (slot < 28 ? 0u : OOB)), [vg0] "v"(vg + kbof(2 * K) * 4096), [vg1] "v"(vg + kbof(2 * K + 1) * 4096), [c0] "v"(2 * (lane & 2)), [c1] "v"((lane & 4) ? 0u : 0xf000f000u), [idx] "v"(half ? 0xEEEE : 0x4444), [sel] "v"(sel), \
    [vwa] "v"(wofs(tA)), [vwb] "v"(wofs(tA + 2)), [vtab] "v"(F_TAB + lane * 128), \
    [vpe] "v"(q * 128 + ((lane & 1) ? OOB : lane * 2)) : CLOB
  if (K == 0) asm volatile(
    "s_mov_b32 s98, m0\n\t"
    "s_getpc_b64 s[48:49]\n\t"
    ".Lwp_%=:\n\t"
    "v_mbcnt_lo_u32_b32 v24, -1, 0\n\t"
    "v_mbcnt_hi_u32_b32 v24, -1, v24\n\t"
    "v_lshlrev_b32 v24, 7, v24\n\t"
    "v_min_u32 v24, .Lend_%=-.Lwp_%=, v24\n\t"
    "global_load_dword v24, v24, s[48:49]\n\t"
    "s_getpc_b64 s[48:49]\n\t"
    ".Lpc_%=:\n\t"
    "s_mov_b32 s50, 0\n\t"
    "s_sub_u32 s50, .Lsl2_%=-.Lpc_%=, s50\n\t"
    "s_add_u32 s52, s48, s50\n\t"
    "s_addc_u32 s53, s49, 0\n\t"
    "s_mov_b32 s46, 0xff00ff00\n\t"
    "s_mov_b32 s47, 0xff00ff00\n\t"
    "s_mov_b32 s41, 0\n\t"
    "s_mov_b32 s42, 0xf000f000\n\t"
    "s_mov_b32 s43, 0x0fff0fff\n\t"
    "s_mov_b32 s44, 0x03010c0c\n\t"
    "v_mov_b32 v104, 0x3f803f80\n\t"
    "v_mov_b32 v105, 0x3f803f80\n\t"
    "v_mov_b32 v106, 0x3f803f80\n\t"
    "v_mov_b32 v107, 0x3f803f80\n\t"
    "s_mov_b32 s50, 512\n\t"
    "v_mov_b32 v81, s50\n\t"
    "v_mov_b32 v80, %[lb]\n\t"
    "v_cndmask_b32_e64 v37, -1, 0, s[46:47]\n\t"
    "v_cndmask_b32_e64 v38, 0, -1, s[46:47]\n\t"
    "s_mov_b32 s99, 0x030c020c\n\t"
    "s_mov_b32 s40, 0xbb800000\n\t"
    "v_mov_b32 v109, 0x32003200\n\t"
    "s_mov_b32 s45, 0\n\t"
    "s_mov_b32 s64, 144\n\t"
    "s_mov_b32 s65, 16\n\t"
    "ds_write_b32 v80, v81 offset:14336\n\t"
    "s_waitcnt lgkmcnt(0)\n\t"
    "s_barrier\n\t"
    "s_barrier\n\t"
    "v_mov_b32 v82, 0\n\t"
    "s_mov_b32 s96, 0\n\t"
    "s_mov_b32 s97, %[lb]\n\t"
    "s_mov_b32 s41, 0\n\t"
    "s_mov_b32 s36, 0\n\t"
    "s_mov_b32 s37, 0\n\t"
    "s_mov_b64 exec, 0\n\t"
    "s_branch .Lloop_%=\n\t"
    ".Lp2_%=:\n\t"
    "s_mov_b64 exec, -1\n\t"
    "s_mov_b32 s96, 0\n\t"
    "s_mov_b32 s97, %[lb]\n\t"
    "s_mov_b32 s41, 0\n\t"
    "s_movk_i32 s45, 1300\n\t"
    "s_add_u32 s50, %[oR], 0\n\t"
    "buffer_load_dwordx4 v[128:131], %[vwa], %[rs], s50 offen offset:0\n\t"
    "buffer_load_dwordx4 v[132:135], %[vwa], %[rs], s50 offen offset:1024\n\t"
    "s_add_u32 s50, %[oR], 2048\n\t"
    "buffer_load_dwordx4 v[136:139], %[vwa], %[rs], s50 offen offset:0\n\t"
    "buffer_load_dwordx4 v[140:143], %[vwa], %[rs], s50 offen offset:1024\n\t"
    "s_add_u32 s50, %[oR], 4096\n\t"
    "buffer_load_dwordx4 v[144:147], %[vwa], %[rs], s50 offen offset:0\n\t"
    "buffer_load_dwordx4 v[148:151], %[vwa], %[rs], s50 offen offset:1024\n\t"
    "s_add_u32 s50, %[oL], 0\n\t"
    "buffer_load_dwordx4 v[152:155], %[vwa], %[rs], s50 offen offset:0\n\t"
    "buffer_load_dwordx4 v[156:159], %[vwa], %[rs], s50 offen offset:1024\n\t"
    "s_add_u32 s50, %[oL], 2048\n\t"
    "buffer_load_dwordx4 v[160:163], %[vwa], %[rs], s50 offen offset:0\n\t"
    "buffer_load_dwordx4 v[164:167], %[vwa], %[rs], s50 offen offset:1024\n\t"
    "s_add_u32 s50, %[oL], 4096\n\t"
    "buffer_load_dwordx4 v[168:171], %[vwa], %[rs], s50 offen offset:0\n\t"
    "buffer_load_dwordx4 v[172:175], %[vwa], %[rs], s50 offen offset:1024\n\t"
    "s_add_u32 s50, %[oM], 0\n\t"
    "buffer_load_dwordx4 v[176:179], %[vwa], %[rs], s50 offen offset:0\n\t"
    "buffer_load_dwordx4 v[180:183], %[vwa], %[rs], s50 offen offset:1024\n\t"
    "s_add_u32 s50, %[oR], 0\n\t"
    "buffer_load_dwordx4 v[184:187], %[vwb], %[rs], s50 offen offset:0\n\t"
    "buffer_load_dwordx4 v[188:191], %[vwb], %[rs], s50 offen offset:1024\n\t"
    "s_add_u32 s50, %[oR], 2048\n\t"
    "buffer_load_dwordx4 v[192:195], %[vwb], %[rs], s50 offen offset:0\n\t"
    "buffer_load_dwordx4 v[196:199], %[vwb], %[rs], s50 offen offset:1024\n\t"
    "s_add_u32 s50, %[oR], 4096\n\t"
    "buffer_load_dwordx4 v[200:203], %[vwb], %[rs], s50 offen offset:0\n\t"
    "buffer_load_dwordx4 v[204:207], %[vwb], %[rs], s50 offen offset:1024\n\t"
    "s_add_u32 s50, %[oL], 0\n\t"
    "buffer_load_dwordx4 v[208:211], %[vwb], %[rs], s50 offen offset:0\n\t"
    "buffer_load_dwordx4 v[212:215], %[vwb], %[rs], s50 offen offset:1024\n\t"
    "s_add_u32 s50, %[oL], 2048\n\t"
    "buffer_load_dwordx4 v[216:219], %[vwb], %[rs], s50 offen offset:0\n\t"
    "buffer_load_dwordx4 v[220:223], %[vwb], %[rs], s50 offen offset:1024\n\t"
    "s_add_u32 s50, %[oL], 4096\n\t"
    "buffer_load_dwordx4 v[224:227], %[vwb], %[rs], s50 offen offset:0\n\t"
    "buffer_load_dwordx4 v[228:231], %[vwb], %[rs], s50 offen offset:1024\n\t"
    "s_add_u32 s50, %[oM], 0\n\t"
    "buffer_load_dwordx4 v[232:235], %[vwb], %[rs], s50 offen offset:0\n\t"
    "buffer_load_dwordx4 v[236:239], %[vwb], %[rs], s50 offen offset:1024\n\t"
    ".Ltb_%=:\n\t"
    "buffer_load_dword v29, %[vtab], %[rs], 0 offen sc1\n\t"
    "s_waitcnt vmcnt(0)\n\t"
    "v_cmp_eq_u32 vcc, 0, v29\n\t"
    "s_cbranch_vccz .Ltq_%=\n\t"
    "s_sub_u32 %[cap], %[cap], 1\n\t"
    "s_cbranch_scc0 .Ltb_%=\n\t"
    "s_mov_b32 %[cap], 0\n\t"
    ".Ltq_%=:\n\t"
    "v_and_b32 v29, -2, v29\n\t"
    "s_mov_b32 s36, 0\n\t"
    "s_nop 0\n\t"
    "v_readlane_b32 s37, v29, 0\n\t"
    "s_mov_b32 s36, 0\n\t"
    "v_readlane_b32 s37, v29, 0\n\t"
    "s_barrier\n\t"
    "s_add_u32 m0, s97, 40960\n\t"
    "s_nop 0\n\t"
    "buffer_load_dwordx4 %[vg0], %[rs], s96 offen sc1 lds\n\t"
    "s_add_u32 m0, s97, 41984\n\t"
    "s_nop 0\n\t"
    "buffer_load_dwordx4 %[vg1], %[rs], s96 offen sc1 lds\n\t"
    "s_add_u32 s96, s96, 458752\n\t"
    "s_add_u32 s97, s97, 0x4000\n\t"
    "s_add_u32 m0, s97, 40960\n\t"
    "s_nop 0\n\t"
    "buffer_load_dwordx4 %[vg0], %[rs], s96 offen sc1 lds\n\t"
    "s_add_u32 m0, s97, 41984\n\t"
    "s_nop 0\n\t"
    "buffer_load_dwordx4 %[vg1], %[rs], s96 offen sc1 lds\n\t"
    "s_add_u32 s96, s96, 458752\n\t"
    "s_setprio 3\n\t"
    "s_waitcnt vmcnt(0)\n\t"
    "s_barrier\n\t"
    ".Lloop_%=:\n\t"
    "s_add_u32 s38, s37, 0x8000\n\t"
    "s_add_u32 s39, s37, 0x10000\n\t"
    "v_add_u32 v30, s41, %[ll]\n\t"
    "v_mov_b64 v[60:61], 0\n\t"
    "v_mov_b64 v[62:63], 0\n\t"
    "v_mov_b64 v[64:65], 0\n\t"
    "v_mov_b64 v[66:67], 0\n\t"
    "s_xor_b32 s97, s41, 0x8000\n\t"
    "s_add_u32 s97, s97, %[lb]\n\t"
    "s_add_u32 s41, s41, 0x4000\n\t"
    "s_and_b32 s41, s41, 0xffff\n\t"
    "s_setprio 3\n\t"
    "s_barrier\n\t"
    "s_memtime s[58:59]\n\t"
    ".Lim_%=:\n\t"
    "buffer_load_dwordx4 v[72:75], %[vm0], %[rs], s37 offen sc1\n\t"
    "s_nop 7\n\t"
    "buffer_load_dwordx4 v[76:79], %[vm1], %[rs], s37 offen sc1\n\t"
    "ds_read_b128 v[100:103], v30 offset:32768\n\t"
    "s_waitcnt vmcnt(1) lgkmcnt(0)\n\t"
    "ds_write_b128 %[lf], v[72:75] offset:3072\n\t"
    "v_smfmac_f32_16x16x64_bf16 v[60:63], v[72:75], v[152:159], %[idx]\n\t"
    "v_max3_u32 v24, v72, v73, v74\n\t"
    "v_smfmac_f32_16x16x64_bf16 v[64:67], v[72:75], v[208:215], %[idx]\n\t"
    "s_waitcnt vmcnt(0)\n\t"
    "ds_write_b128 %[lf], v[76:79] offset:0\n\t"
    "v_max3_u32 v28, v76, v77, v78\n\t"
    "v_max3_u32 v24, v24, v75, v79\n\t"
    "v_smfmac_f32_16x16x64_bf16 v[60:63], v[76:79], v[128:135], %[idx]\n\t"
    "v_max_u32 v28, v28, v24\n\t"
    "v_cmp_eq_u32 vcc, -1, v28\n\t"
    "v_smfmac_f32_16x16x64_bf16 v[64:67], v[76:79], v[184:191], %[idx]\n\t"
    "s_cbranch_vccnz .Lfm_%=\n\t"
    ".Lgd_%=:\n\t"
    "s_waitcnt lgkmcnt(0)\n\t"
    "s_barrier\n\t"
    "s_nop 4\n\t"
    "ds_read_b128 v[40:43], %[lf] offset:1024\n\t"
    "ds_read_b128 v[44:47], %[lf] offset:2048\n\t"
    "ds_read_b128 v[48:51], %[lf] offset:4096\n\t"
    "ds_read_b128 v[52:55], %[lf] offset:5120\n\t"
    "ds_read_b128 v[56:59], %[lf] offset:6144\n\t"
    "s_waitcnt lgkmcnt(4)\n\t"
    "v_smfmac_f32_16x16x64_bf16 v[60:63], v[40:43], v[136:143], %[idx]\n\t"
    "v_smfmac_f32_16x16x64_bf16 v[64:67], v[40:43], v[192:199], %[idx]\n\t"
    "s_waitcnt lgkmcnt(3)\n\t"
    "v_smfmac_f32_16x16x64_bf16 v[60:63], v[44:47], v[144:151], %[idx]\n\t"
    "v_smfmac_f32_16x16x64_bf16 v[64:67], v[44:47], v[200:207], %[idx]\n\t"
    "s_waitcnt lgkmcnt(2)\n\t"
    "v_smfmac_f32_16x16x64_bf16 v[60:63], v[48:51], v[160:167], %[idx]\n\t"
    "s_waitcnt lgkmcnt(1)\n\t"
    "v_smfmac_f32_16x16x64_bf16 v[60:63], v[52:55], v[168:175], %[idx]\n\t"
    "s_waitcnt lgkmcnt(0)\n\t"
    "v_smfmac_f32_16x16x64_bf16 v[60:63], v[56:59], v[176:183], %[idx]\n\t"
    "v_smfmac_f32_16x16x64_bf16 v[64:67], v[48:51], v[216:223], %[idx]\n\t"
    "v_smfmac_f32_16x16x64_bf16 v[64:67], v[52:55], v[224:231], %[idx]\n\t"
    "v_smfmac_f32_16x16x64_bf16 v[64:67], v[56:59], v[232:239], %[idx]\n\t"
    "s_nop 4\n\t"
    "v_add_f32 v60, v60, v62\n\t"
    "v_add_f32 v61, v61, v63\n\t"
    "v_cvt_pk_bf16_f32 v24, v60, v61\n\t"
    "v_add_f32 v64, v64, v66\n\t"
    "v_add_f32 v65, v65, v67\n\t"
    "v_cvt_pk_bf16_f32 v25, v64, v65\n\t"
    "ds_write_b64 %[limg], v[24:25]\n\t"
    "s_waitcnt lgkmcnt(0)\n\t"
    "s_barrier\n\t"
    "s_memtime s[60:61]\n\t"
    "ds_read_b32 v82, v80 offset:14336\n\t"
    "v_mov_b32 v83, 0\n\t"
    "s_add_u32 s36, s36, 1\n\t"
    "s_cmp_eq_u32 s36, 64\n\t"
    "s_cbranch_scc1 .Lend_%=\n\t"
    "s_add_u32 s58, s58, s45\n\t"
    "s_waitcnt lgkmcnt(0)\n\t"
    "v_readfirstlane_b32 s50, v82\n\t"
    "ds_write_b32 v80, v83 offset:14336\n\t"
    "s_sub_u32 s59, s58, s60\n\t"
    "s_add_u32 s51, s59, s64\n\t"
    "s_cmp_lg_u32 s50, 0\n\t"
    "s_cselect_b32 s59, 0, s59\n\t"
    "s_add_u32 s50, s50, s65\n\t"
    "s_max_i32 s50, s50, s51\n\t"
    "s_min_u32 s50, s50, 768\n\t"
    "s_lshr_b32 s50, s50, 1\n\t"
    "s_and_b32 s50, s50, -4\n\t"
    "s_sub_u32 s48, s52, s50\n\t"
    "s_subb_u32 s49, s53, 0\n\t"
    "s_setpc_b64 s[48:49]\n\t"
    ".rept 96\n\t"
    "s_nop 1\n\t"
    ".endr\n\t"
    ".Lsl2_%=:\n\t"
    "s_barrier\n\t"
    "buffer_load_dwordx4 v[72:75], %[vl], %[rs], s39 offen sc1\n\t"
    "s_setprio 3\n\t"
    "s_max_i32 s59, s59, -128\n\t"
    "s_min_i32 s59, s59, 128\n\t"
    "s_ashr_i32 s59, s59, 2\n\t"
    "s_sub_u32 s45, s45, s59\n\t"
    "s_nop 15\n\t"
    "s_nop 15\n\t"
    "s_nop 15\n\t"
    "buffer_load_dwordx4 v[76:79], %[vl], %[rs], s38 offen sc1\n\t"
    "s_barrier\n\t"
    "ds_read_b64 v[96:97], %[ll] offset:10240\n\t"
    "v_readlane_b32 s37, v29, s36\n\t"
    "s_add_u32 m0, s97, 40960\n\t"
    "s_nop 0\n\t"
    "buffer_load_dwordx4 %[vg0], %[rs], s96 offen sc1 lds\n\t"
    "s_add_u32 m0, s97, 41984\n\t"
    "s_nop 0\n\t"
    "buffer_load_dwordx4 %[vg1], %[rs], s96 offen sc1 lds\n\t"
    "s_add_u32 s96, s96, 458752\n\t"
    "s_waitcnt vmcnt(3) lgkmcnt(0)\n\t"
    ".Lme_%=:\n\t"
    "v_lshrrev_b32 v26, 4, v73\n\t"
    "v_bfi_b32 v26, s42, v72, v26\n\t"
    "v_perm_b32 v24, v26, v26, s44\n\t"
    "v_perm_b32 v25, v26, v26, s44\n\t"
    "v_perm_b32 v39, v26, v26, s44\n\t"
    "v_max3_u32 v28, v72, v73, v74\n\t"
    "v_permlane16_swap_b32 v24, v25\n\t"
    "v_max_f32 v26, v24, v25\n\t"
    "v_max_f32 v27, v24, v25\n\t"
    "v_cmp_u_f32 vcc, v28, v75\n\t"
    "s_nop 0\n\t"
    "v_permlane32_swap_b32 v26, v27\n\t"
    "s_cbranch_vccnz .Lfe_%=\n\t"
    ".Leg_%=:\n\t"
    "v_max_f32 v31, v26, v27\n\t"
    "v_sub_f32 v24, v39, v31\n\t"
    "v_add_f32 v24, 0x42c80000, v24\n\t"
    "v_cvt_u32_f32 v24, v24\n\t"
    "v_and_b32 v72, s43, v72\n\t"
    "v_mul_u32_u24 v24, 0x800080, v24\n\t"
    "v_and_b32 v73, s43, v73\n\t"
    "v_add_u32 v74, v74, v24\n\t"
    "v_add_u32 v75, v75, v24\n\t"
    "v_add_u32 v72, v72, v24\n\t"
    "v_add_u32 v73, v73, v24\n\t"
    "s_nop 1\n\t"
    "v_mfma_f32_16x16x32_bf16 v[40:43], v[100:103], v[72:75], 0\n\t"
    "v_mfma_f32_16x16x32_bf16 v[44:47], v[104:107], v[72:75], 0\n\t"
    "s_waitcnt vmcnt(2)\n\t"
    ".Lms_%=:\n\t"
    "v_mfma_f32_16x16x32_bf16 v[48:51], %[sel], v[76:79], 0\n\t"
    "v_max3_u32 v28, v76, v77, v78\n\t"
    "v_cmp_u_f32 vcc, v28, v79\n\t"
    "s_nop 1\n\t"
    "s_cbranch_vccnz .Lfs_%=\n\t"
    ".Lsg_%=:\n\t"
    "s_nop 0\n\t"
    "v_cndmask_b32_e64 v40, v40, v42, s[46:47]\n\t"
    "v_cndmask_b32_e64 v41, v41, v43, s[46:47]\n\t"
    "v_max3_f32 v24, v48, v49, v50\n\t"
    "v_max3_f32 v25, v24, v51, v31\n\t"
    "v_max3_f32 v24, v24, v51, v31\n\t"
    "s_nop 1\n\t"
    "v_permlane32_swap_b32 v24, v25\n\t"
    "v_max_f32 v32, v24, v25\n\t"
    "v_sub_f32 v48, v48, v32\n\t"
    "v_sub_f32 v49, v49, v32\n\t"
    "v_exp_f32 v48, v48\n\t"
    "v_exp_f32 v49, v49\n\t"
    "v_sub_f32 v36, v31, v32\n\t"
    "v_cvt_pk_bf16_f32 v33, v48, v49\n\t"
    "v_and_b32 v34, v33, v37\n\t"
    "v_and_b32 v35, v33, v38\n\t"
    "s_nop 1\n\t"
    "v_mfma_f32_16x16x16_bf16 v[52:55], v[96:97], v[34:35], 0\n\t"
    "v_exp_f32 v36, v36\n\t"
    "s_nop 6\n\t"
    "v_fma_f32 v24, v44, v36, v54\n\t"
    "v_rcp_f32 v24, v24\n\t"
    "v_fma_f32 v52, v40, v36, v52\n\t"
    "v_fma_f32 v53, v41, v36, v53\n\t"
    "v_mul_f32 v52, v52, v24\n\t"
    "v_mul_f32 v53, v53, v24\n\t"
    "s_nop 0\n\t"
    "v_add_f32_dpp v52, v52, v52 row_ror:8 row_mask:0xf bank_mask:0xf\n\t"
    "v_add_f32_dpp v53, v53, v53 row_ror:8 row_mask:0xf bank_mask:0xf\n\t"
    "v_cvt_pk_bf16_f32 v24, v52, v53\n\t"
    "ds_write_b32 %[lred], v24\n\t"
    "s_waitcnt lgkmcnt(0)\n\t"
    "s_barrier\n\t"
    "s_cbranch_execnz .Lloop_%=\n\t"
    "s_branch .Lp2_%=\n\t"
    "s_nop 7\n\t"
    ".Lfm_%=:\n\t"
    "ds_write_b32 v80, v81 offset:14336\n\t"
    "s_sub_u32 %[cap], %[cap], 1\n\t"
    "s_cbranch_scc1 .Ldm_%=\n\t"
    "s_nop 5\n\t"
    "v_mov_b64 v[60:61], 0\n\t"
    "v_mov_b64 v[62:63], 0\n\t"
    "v_mov_b64 v[64:65], 0\n\t"
    "v_mov_b64 v[66:67], 0\n\t"
    "s_branch .Lim_%=\n\t"
    ".Ldm_%=:\n\t"
    "s_mov_b32 %[cap], 0\n\t"
    "s_branch .Lgd_%=\n\t"
    ".Lfe_%=:\n\t"
    "v_max3_u32 v28, v72, v73, v74\n\t"
    "v_max_u32 v28, v28, v75\n\t"
    "v_cmp_eq_u32 vcc, -1, v28\n\t"
    "s_nop 0\n\t"
    "s_cbranch_vccz .Leg_%=\n\t"
    "ds_write_b32 v80, v81 offset:14336\n\t"
    "s_sub_u32 %[cap], %[cap], 1\n\t"
    "s_cbranch_scc1 .Lde_%=\n\t"
    "buffer_load_dwordx4 v[72:75], %[vl], %[rs], s39 offen sc1\n\t"
    "buffer_load_dwordx4 v[76:79], %[vl], %[rs], s38 offen sc1\n\t"
    "s_waitcnt vmcnt(0)\n\t"
    "s_branch .Lme_%=\n\t"
    ".Lde_%=:\n\t"
    "s_mov_b32 %[cap], 0\n\t"
    "s_branch .Leg_%=\n\t"
    ".Lfs_%=:\n\t"
    "v_max3_u32 v28, v76, v77, v78\n\t"
    "v_max_u32 v28, v28, v79\n\t"
    "v_cmp_eq_u32 vcc, -1, v28\n\t"
    "s_nop 0\n\t"
    "s_cbranch_vccz .Lsg_%=\n\t"
    "ds_write_b32 v80, v81 offset:14336\n\t"
    "s_sub_u32 %[cap], %[cap], 1\n\t"
    "s_cbranch_scc1 .Lds_%=\n\t"
    "buffer_load_dwordx4 v[76:79], %[vl], %[rs], s38 offen sc1\n\t"
    "s_waitcnt vmcnt(0)\n\t"
    "s_branch .Lms_%=\n\t"
    ".Lds_%=:\n\t"
    "s_mov_b32 %[cap], 0\n\t"
    "s_branch .Lsg_%=\n\t"
    ".Lend_%=:\n\t"
    "s_lshl_b32 s50, s45, 20\n\t"
    "s_barrier\n\t"
    "s_barrier\n\t"
    "s_waitcnt vmcnt(0)\n\t"
    "s_setprio 0\n\t"
    "s_mov_b32 m0, s98\n\t"
    MAIN_OPS);
  if (K == 1) asm volatile(
    "s_mov_b32 s98, m0\n\t"
    "s_getpc_b64 s[48:49]\n\t"
    ".Lwp_%=:\n\t"
    "v_mbcnt_lo_u32_b32 v24, -1, 0\n\t"
    "v_mbcnt_hi_u32_b32 v24, -1, v24\n\t"
    "v_lshlrev_b32 v24, 7, v24\n\t"
    "v_min_u32 v24, .Lend_%=-.Lwp_%=, v24\n\t"
    "global_load_dword v24, v24, s[48:49]\n\t"
    "s_mov_b32 s46, 0xff00ff00\n\t"
    "s_mov_b32 s47, 0xff00ff00\n\t"
    "s_mov_b32 s41, 0\n\t"
    "s_mov_b32 s42, 0xf000f000\n\t"
    "s_mov_b32 s43, 0x0fff0fff\n\t"
    "s_mov_b32 s44, 0x03010c0c\n\t"
    "v_mov_b32 v104, 0x3f803f80\n\t"
    "v_mov_b32 v105, 0x3f803f80\n\t"
    "v_mov_b32 v106, 0x3f803f80\n\t"
    "v_mov_b32 v107, 0x3f803f80\n\t"
    "s_mov_b32 s50, 512\n\t"
    "v_mov_b32 v81, s50\n\t"
    "v_mov_b32 v80, %[lb]\n\t"
    "v_cndmask_b32_e64 v37, -1, 0, s[46:47]\n\t"
    "v_cndmask_b32_e64 v38, 0, -1, s[46:47]\n\t"
    "s_mov_b32 s99, 0x030c020c\n\t"
    "s_mov_b32 s40, 0xbb800000\n\t"
    "v_mov_b32 v109, 0x32003200\n\t"
    "s_mov_b32 s45, 0\n\t"
    "s_barrier\n\t"
    "s_barrier\n\t"
    "v_mov_b32 v82, 0\n\t"
    "s_mov_b32 s96, 0\n\t"
    "s_mov_b32 s97, %[lb]\n\t"
    "s_mov_b32 s41, 0\n\t"
    "s_mov_b32 s36, 0\n\t"
    "s_mov_b32 s37, 0\n\t"
    "s_mov_b64 exec, 0\n\t"
    "s_branch .Lloop_%=\n\t"
    ".Lp2_%=:\n\t"
    "s_mov_b64 exec, -1\n\t"
    "s_mov_b32 s96, 0\n\t"
    "s_mov_b32 s97, %[lb]\n\t"
    "s_mov_b32 s41, 0\n\t"
    "s_movk_i32 s45, 1300\n\t"
    "s_add_u32 s50, %[oR], 0\n\t"
    "buffer_load_dwordx4 v[128:131], %[vwa], %[rs], s50 offen offset:0\n\t"
    "buffer_load_dwordx4 v[132:135], %[vwa], %[rs], s50 offen offset:1024\n\t"
    "s_add_u32 s50, %[oR], 2048\n\t"
    "buffer_load_dwordx4 v[136:139], %[vwa], %[rs], s50 offen offset:0\n\t"
    "buffer_load_dwordx4 v[140:143], %[vwa], %[rs], s50 offen offset:1024\n\t"
    "s_add_u32 s50, %[oR], 4096\n\t"
    "buffer_load_dwordx4 v[144:147], %[vwa], %[rs], s50 offen offset:0\n\t"
    "buffer_load_dwordx4 v[148:151], %[vwa], %[rs], s50 offen offset:1024\n\t"
    "s_add_u32 s50, %[oL], 0\n\t"
    "buffer_load_dwordx4 v[152:155], %[vwa], %[rs], s50 offen offset:0\n\t"
    "buffer_load_dwordx4 v[156:159], %[vwa], %[rs], s50 offen offset:1024\n\t"
    "s_add_u32 s50, %[oL], 2048\n\t"
    "buffer_load_dwordx4 v[160:163], %[vwa], %[rs], s50 offen offset:0\n\t"
    "buffer_load_dwordx4 v[164:167], %[vwa], %[rs], s50 offen offset:1024\n\t"
    "s_add_u32 s50, %[oL], 4096\n\t"
    "buffer_load_dwordx4 v[168:171], %[vwa], %[rs], s50 offen offset:0\n\t"
    "buffer_load_dwordx4 v[172:175], %[vwa], %[rs], s50 offen offset:1024\n\t"
    "s_add_u32 s50, %[oM], 0\n\t"
    "buffer_load_dwordx4 v[176:179], %[vwa], %[rs], s50 offen offset:0\n\t"
    "buffer_load_dwordx4 v[180:183], %[vwa], %[rs], s50 offen offset:1024\n\t"
    "s_add_u32 s50, %[oR], 0\n\t"
    "buffer_load_dwordx4 v[184:187], %[vwb], %[rs], s50 offen offset:0\n\t"
    "buffer_load_dwordx4 v[188:191], %[vwb], %[rs], s50 offen offset:1024\n\t"
    "s_add_u32 s50, %[oR], 2048\n\t"
    "buffer_load_dwordx4 v[192:195], %[vwb], %[rs], s50 offen offset:0\n\t"
    "buffer_load_dwordx4 v[196:199], %[vwb], %[rs], s50 offen offset:1024\n\t"
    "s_add_u32 s50, %[oR], 4096\n\t"
    "buffer_load_dwordx4 v[200:203], %[vwb], %[rs], s50 offen offset:0\n\t"
    "buffer_load_dwordx4 v[204:207], %[vwb], %[rs], s50 offen offset:1024\n\t"
    "s_add_u32 s50, %[oL], 0\n\t"
    "buffer_load_dwordx4 v[208:211], %[vwb], %[rs], s50 offen offset:0\n\t"
    "buffer_load_dwordx4 v[212:215], %[vwb], %[rs], s50 offen offset:1024\n\t"
    "s_add_u32 s50, %[oL], 2048\n\t"
    "buffer_load_dwordx4 v[216:219], %[vwb], %[rs], s50 offen offset:0\n\t"
    "buffer_load_dwordx4 v[220:223], %[vwb], %[rs], s50 offen offset:1024\n\t"
    "s_add_u32 s50, %[oL], 4096\n\t"
    "buffer_load_dwordx4 v[224:227], %[vwb], %[rs], s50 offen offset:0\n\t"
    "buffer_load_dwordx4 v[228:231], %[vwb], %[rs], s50 offen offset:1024\n\t"
    "s_add_u32 s50, %[oM], 0\n\t"
    "buffer_load_dwordx4 v[232:235], %[vwb], %[rs], s50 offen offset:0\n\t"
    "buffer_load_dwordx4 v[236:239], %[vwb], %[rs], s50 offen offset:1024\n\t"
    ".Ltb_%=:\n\t"
    "buffer_load_dword v29, %[vtab], %[rs], 0 offen sc1\n\t"
    "s_waitcnt vmcnt(0)\n\t"
    "v_cmp_eq_u32 vcc, 0, v29\n\t"
    "s_cbranch_vccz .Ltq_%=\n\t"
    "s_sub_u32 %[cap], %[cap], 1\n\t"
    "s_cbranch_scc0 .Ltb_%=\n\t"
    "s_mov_b32 %[cap], 0\n\t"
    ".Ltq_%=:\n\t"
    "v_and_b32 v29, -2, v29\n\t"
    "s_mov_b32 s36, 0\n\t"
    "s_nop 0\n\t"
    "v_readlane_b32 s37, v29, 0\n\t"
    "s_mov_b32 s36, 0\n\t"
    "v_readlane_b32 s37, v29, 0\n\t"
    "s_barrier\n\t"
    "s_add_u32 m0, s97, 43008\n\t"
    "s_nop 0\n\t"
    "buffer_load_dwordx4 %[vg0], %[rs], s96 offen sc1 lds\n\t"
    "s_add_u32 m0, s97, 44032\n\t"
    "s_nop 0\n\t"
    "buffer_load_dwordx4 %[vg1], %[rs], s96 offen sc1 lds\n\t"
    "s_add_u32 s96, s96, 458752\n\t"
    "s_add_u32 s97, s97, 0x4000\n\t"
    "s_add_u32 m0, s97, 43008\n\t"
    "s_nop 0\n\t"
    "buffer_load_dwordx4 %[vg0], %[rs], s96 offen sc1 lds\n\t"
    "s_add_u32 m0, s97, 44032\n\t"
    "s_nop 0\n\t"
    "buffer_load_dwordx4 %[vg1], %[rs], s96 offen sc1 lds\n\t"
    "s_add_u32 s96, s96, 458752\n\t"
    "s_setprio 3\n\t"
    "s_waitcnt vmcnt(0)\n\t"
    "s_barrier\n\t"
    ".Lloop_%=:\n\t"
    "s_add_u32 s38, s37, 0x8000\n\t"
    "s_add_u32 s39, s37, 0x10000\n\t"
    "v_add_u32 v30, s41, %[ll]\n\t"
    "v_mov_b64 v[60:61], 0\n\t"
    "v_mov_b64 v[62:63], 0\n\t"
    "v_mov_b64 v[64:65], 0\n\t"
    "v_mov_b64 v[66:67], 0\n\t"
    "s_xor_b32 s97, s41, 0x8000\n\t"
    "s_add_u32 s97, s97, %[lb]\n\t"
    "s_add_u32 s41, s41, 0x4000\n\t"
    "s_and_b32 s41, s41, 0xffff\n\t"
    "s_setprio 3\n\t"
    "s_barrier\n\t"
    ".Lim_%=:\n\t"
    "buffer_load_dwordx4 v[72:75], %[vm0], %[rs], s37 offen sc1\n\t"
    "s_nop 7\n\t"
    "buffer_load_dwordx4 v[76:79], %[vm1], %[rs], s37 offen sc1\n\t"
    "ds_read_b128 v[100:103], v30 offset:34816\n\t"
    "s_waitcnt vmcnt(1) lgkmcnt(0)\n\t"
    "ds_write_b128 %[lf], v[72:75] offset:4096\n\t"
    "v_smfmac_f32_16x16x64_bf16 v[60:63], v[72:75], v[160:167], %[idx]\n\t"
    "v_max3_u32 v24, v72, v73, v74\n\t"
    "v_smfmac_f32_16x16x64_bf16 v[64:67], v[72:75], v[216:223], %[idx]\n\t"
    "s_waitcnt vmcnt(0)\n\t"
    "ds_write_b128 %[lf], v[76:79] offset:1024\n\t"
    "v_max3_u32 v28, v76, v77, v78\n\t"
    "v_max3_u32 v24, v24, v75, v79\n\t"
    "v_smfmac_f32_16x16x64_bf16 v[60:63], v[76:79], v[136:143], %[idx]\n\t"
    "v_max_u32 v28, v28, v24\n\t"
    "v_cmp_eq_u32 vcc, -1, v28\n\t"
    "v_smfmac_f32_16x16x64_bf16 v[64:67], v[76:79], v[192:199], %[idx]\n\t"
    "s_cbranch_vccnz .Lfm_%=\n\t"
    ".Lgd_%=:\n\t"
    "s_waitcnt lgkmcnt(0)\n\t"
    "s_barrier\n\t"
    "s_nop 4\n\t"
    "ds_read_b128 v[40:43], %[lf] offset:0\n\t"
    "ds_read_b128 v[44:47], %[lf] offset:2048\n\t"
    "ds_read_b128 v[48:51], %[lf] offset:3072\n\t"
    "ds_read_b128 v[52:55], %[lf] offset:5120\n\t"
    "ds_read_b128 v[56:59], %[lf] offset:6144\n\t"
    "s_waitcnt lgkmcnt(4)\n\t"
    "v_smfmac_f32_16x16x64_bf16 v[60:63], v[40:43], v[128:135], %[idx]\n\t"
    "v_smfmac_f32_16x16x64_bf16 v[64:67], v[40:43], v[184:191], %[idx]\n\t"
    "s_waitcnt lgkmcnt(3)\n\t"
    "v_smfmac_f32_16x16x64_bf16 v[60:63], v[44:47], v[144:151], %[idx]\n\t"
    "v_smfmac_f32_16x16x64_bf16 v[64:67], v[44:47], v[200:207], %[idx]\n\t"
    "s_waitcnt lgkmcnt(2)\n\t"
    "v_smfmac_f32_16x16x64_bf16 v[60:63], v[48:51], v[152:159], %[idx]\n\t"
    "s_waitcnt lgkmcnt(1)\n\t"
    "v_smfmac_f32_16x16x64_bf16 v[60:63], v[52:55], v[168:175], %[idx]\n\t"
    "s_waitcnt lgkmcnt(0)\n\t"
    "v_smfmac_f32_16x16x64_bf16 v[60:63], v[56:59], v[176:183], %[idx]\n\t"
    "v_smfmac_f32_16x16x64_bf16 v[64:67], v[48:51], v[208:215], %[idx]\n\t"
    "v_smfmac_f32_16x16x64_bf16 v[64:67], v[52:55], v[224:231], %[idx]\n\t"
    "v_smfmac_f32_16x16x64_bf16 v[64:67], v[56:59], v[232:239], %[idx]\n\t"
    "s_nop 4\n\t"
    "v_add_f32 v60, v60, v62\n\t"
    "v_add_f32 v61, v61, v63\n\t"
    "v_cvt_pk_bf16_f32 v24, v60, v61\n\t"
    "v_add_f32 v64, v64, v66\n\t"
    "v_add_f32 v65, v65, v67\n\t"
    "v_cvt_pk_bf16_f32 v25, v64, v65\n\t"
    "ds_write_b64 %[limg], v[24:25]\n\t"
    "s_waitcnt lgkmcnt(0)\n\t"
    "s_barrier\n\t"
    "s_add_u32 s36, s36, 1\n\t"
    "s_cmp_eq_u32 s36, 64\n\t"
    "s_cbranch_scc1 .Lend_%=\n\t"
    "s_barrier\n\t"
    "buffer_load_dwordx4 v[72:75], %[vl], %[rs], s39 offen sc1\n\t"
    "s_setprio 3\n\t"
    "s_nop 3\n\t"
    "s_nop 15\n\t"
    "s_nop 15\n\t"
    "s_nop 15\n\t"
    "buffer_load_dwordx4 v[76:79], %[vl], %[rs], s38 offen sc1\n\t"
    "s_barrier\n\t"
    "ds_read_b64 v[96:97], %[ll] offset:11264\n\t"
    "v_readlane_b32 s37, v29, s36\n\t"
    "s_add_u32 m0, s97, 43008\n\t"
    "s_nop 0\n\t"
    "buffer_load_dwordx4 %[vg0], %[rs], s96 offen sc1 lds\n\t"
    "s_add_u32 m0, s97, 44032\n\t"
    "s_nop 0\n\t"
    "buffer_load_dwordx4 %[vg1], %[rs], s96 offen sc1 lds\n\t"
    "s_add_u32 s96, s96, 458752\n\t"
    "s_waitcnt vmcnt(3) lgkmcnt(0)\n\t"
    ".Lme_%=:\n\t"
    "v_lshrrev_b32 v26, 4, v73\n\t"
    "v_bfi_b32 v26, s42, v72, v26\n\t"
    "v_perm_b32 v24, v26, v26, s44\n\t"
    "v_perm_b32 v25, v26, v26, s44\n\t"
    "v_perm_b32 v39, v26, v26, s44\n\t"
    "v_max3_u32 v28, v72, v73, v74\n\t"
    "v_permlane16_swap_b32 v24, v25\n\t"
    "v_max_f32 v26, v24, v25\n\t"
    "v_max_f32 v27, v24, v25\n\t"
    "v_cmp_u_f32 vcc, v28, v75\n\t"
    "s_nop 0\n\t"
    "v_permlane32_swap_b32 v26, v27\n\t"
    "s_cbranch_vccnz .Lfe_%=\n\t"
    ".Leg_%=:\n\t"
    "v_max_f32 v31, v26, v27\n\t"
    "v_sub_f32 v24, v39, v31\n\t"
    "v_add_f32 v24, 0x42c80000, v24\n\t"
    "v_cvt_u32_f32 v24, v24\n\t"
    "v_and_b32 v72, s43, v72\n\t"
    "v_mul_u32_u24 v24, 0x800080, v24\n\t"
    "v_and_b32 v73, s43, v73\n\t"
    "v_add_u32 v74, v74, v24\n\t"
    "v_add_u32 v75, v75, v24\n\t"
    "v_add_u32 v72, v72, v24\n\t"
    "v_add_u32 v73, v73, v24\n\t"
    "s_nop 1\n\t"
    "v_mfma_f32_16x16x32_bf16 v[40:43], v[100:103], v[72:75], 0\n\t"
    "v_mfma_f32_16x16x32_bf16 v[44:47], v[104:107], v[72:75], 0\n\t"
    "s_waitcnt vmcnt(2)\n\t"
    ".Lms_%=:\n\t"
    "v_mfma_f32_16x16x32_bf16 v[48:51], %[sel], v[76:79], 0\n\t"
    "v_max3_u32 v28, v76, v77, v78\n\t"
    "v_cmp_u_f32 vcc, v28, v79\n\t"
    "s_nop 1\n\t"
    "s_cbranch_vccnz .Lfs_%=\n\t"
    ".Lsg_%=:\n\t"
    "s_nop 0\n\t"
    "v_cndmask_b32_e64 v40, v40, v42, s[46:47]\n\t"
    "v_cndmask_b32_e64 v41, v41, v43, s[46:47]\n\t"
    "v_max3_f32 v24, v48, v49, v50\n\t"
    "v_max3_f32 v25, v24, v51, v31\n\t"
    "v_max3_f32 v24, v24, v51, v31\n\t"
    "s_nop 1\n\t"
    "v_permlane32_swap_b32 v24, v25\n\t"
    "v_max_f32 v32, v24, v25\n\t"
    "v_sub_f32 v48, v48, v32\n\t"
    "v_sub_f32 v49, v49, v32\n\t"
    "v_exp_f32 v48, v48\n\t"
    "v_exp_f32 v49, v49\n\t"
    "v_sub_f32 v36, v31, v32\n\t"
    "v_cvt_pk_bf16_f32 v33, v48, v49\n\t"
    "v_and_b32 v34, v33, v37\n\t"
    "v_and_b32 v35, v33, v38\n\t"
    "s_nop 1\n\t"
    "v_mfma_f32_16x16x16_bf16 v[52:55], v[96:97], v[34:35], 0\n\t"
    "v_exp_f32 v36, v36\n\t"
    "s_nop 6\n\t"
    "v_fma_f32 v24, v44, v36, v54\n\t"
    "v_rcp_f32 v24, v24\n\t"
    "v_fma_f32 v52, v40, v36, v52\n\t"
    "v_fma_f32 v53, v41, v36, v53\n\t"
    "v_mul_f32 v52, v52, v24\n\t"
    "v_mul_f32 v53, v53, v24\n\t"
    "s_nop 0\n\t"
    "v_add_f32_dpp v52, v52, v52 row_ror:8 row_mask:0xf bank_mask:0xf\n\t"
    "v_add_f32_dpp v53, v53, v53 row_ror:8 row_mask:0xf bank_mask:0xf\n\t"
    "v_cvt_pk_bf16_f32 v24, v52, v53\n\t"
    "ds_write_b32 %[lred], v24\n\t"
    "s_waitcnt lgkmcnt(0)\n\t"
    "s_barrier\n\t"
    "s_cbranch_execnz .Lloop_%=\n\t"
    "s_branch .Lp2_%=\n\t"
    "s_nop 7\n\t"
    ".Lfm_%=:\n\t"
    "ds_write_b32 v80, v81 offset:14336\n\t"
    "s_sub_u32 %[cap], %[cap], 1\n\t"
    "s_cbranch_scc1 .Ldm_%=\n\t"
    "s_nop 5\n\t"
    "v_mov_b64 v[60:61], 0\n\t"
    "v_mov_b64 v[62:63], 0\n\t"
    "v_mov_b64 v[64:65], 0\n\t"
    "v_mov_b64 v[66:67], 0\n\t"
    "s_branch .Lim_%=\n\t"
    ".Ldm_%=:\n\t"
    "s_mov_b32 %[cap], 0\n\t"
    "s_branch .Lgd_%=\n\t"
    ".Lfe_%=:\n\t"
    "v_max3_u32 v28, v72, v73, v74\n\t"
    "v_max_u32 v28, v28, v75\n\t"
    "v_cmp_eq_u32 vcc, -1, v28\n\t"
    "s_nop 0\n\t"
    "s_cbranch_vccz .Leg_%=\n\t"
    "ds_write_b32 v80, v81 offset:14336\n\t"
    "s_sub_u32 %[cap], %[cap], 1\n\t"
    "s_cbranch_scc1 .Lde_%=\n\t"
    "buffer_load_dwordx4 v[72:75], %[vl], %[rs], s39 offen sc1\n\t"
    "buffer_load_dwordx4 v[76:79], %[vl], %[rs], s38 offen sc1\n\t"
    "s_waitcnt vmcnt(0)\n\t"
    "s_branch .Lme_%=\n\t"
    ".Lde_%=:\n\t"
    "s_mov_b32 %[cap], 0\n\t"
    "s_branch .Leg_%=\n\t"
    ".Lfs_%=:\n\t"
    "v_max3_u32 v28, v76, v77, v78\n\t"
    "v_max_u32 v28, v28, v79\n\t"
    "v_cmp_eq_u32 vcc, -1, v28\n\t"
    "s_nop 0\n\t"
    "s_cbranch_vccz .Lsg_%=\n\t"
    "ds_write_b32 v80, v81 offset:14336\n\t"
    "s_sub_u32 %[cap], %[cap], 1\n\t"
    "s_cbranch_scc1 .Lds_%=\n\t"
    "buffer_load_dwordx4 v[76:79], %[vl], %[rs], s38 offen sc1\n\t"
    "s_waitcnt vmcnt(0)\n\t"
    "s_branch .Lms_%=\n\t"
    ".Lds_%=:\n\t"
    "s_mov_b32 %[cap], 0\n\t"
    "s_branch .Lsg_%=\n\t"
    ".Lend_%=:\n\t"
    "s_barrier\n\t"
    "s_barrier\n\t"
    "s_waitcnt vmcnt(0)\n\t"
    "s_setprio 0\n\t"
    "s_mov_b32 m0, s98\n\t"
    MAIN_OPS);
  if (K == 2) asm volatile(
    "s_mov_b32 s98, m0\n\t"
    "s_getpc_b64 s[48:49]\n\t"
    ".Lwp_%=:\n\t"
    "v_mbcnt_lo_u32_b32 v24, -1, 0\n\t"
    "v_mbcnt_hi_u32_b32 v24, -1, v24\n\t"
    "v_lshlrev_b32 v24, 7, v24\n\t"
    "v_min_u32 v24, .Lend_%=-.Lwp_%=, v24\n\t"
    "global_load_dword v24, v24, s[48:49]\n\t"
    "s_mov_b32 s46, 0xff00ff00\n\t"
    "s_mov_b32 s47, 0xff00ff00\n\t"
    "s_mov_b32 s41, 0\n\t"
    "s_mov_b32 s42, 0xf000f000\n\t"
    "s_mov_b32 s43, 0x0fff0fff\n\t"
    "s_mov_b32 s44, 0x03010c0c\n\t"
    "v_mov_b32 v104, 0x3f803f80\n\t"
    "v_mov_b32 v105, 0x3f803f80\n\t"
    "v_mov_b32 v106, 0x3f803f80\n\t"
    "v_mov_b32 v107, 0x3f803f80\n\t"
    "s_mov_b32 s50, 512\n\t"
    "v_mov_b32 v81, s50\n\t"
    "v_mov_b32 v80, %[lb]\n\t"
    "v_cndmask_b32_e64 v37, -1, 0, s[46:47]\n\t"
    "v_cndmask_b32_e64 v38, 0, -1, s[46:47]\n\t"
    "s_mov_b32 s99, 0x030c020c\n\t"
    "s_mov_b32 s40, 0xbb800000\n\t"
    "v_mov_b32 v109, 0x32003200\n\t"
    "s_mov_b32 s45, 0\n\t"
    "s_barrier\n\t"
    "s_barrier\n\t"
    "v_mov_b32 v82, 0\n\t"
    "s_mov_b32 s96, 0\n\t"
    "s_mov_b32 s97, %[lb]\n\t"
    "s_mov_b32 s41, 0\n\t"
    "s_mov_b32 s36, 0\n\t"
    "s_mov_b32 s37, 0\n\t"
    "s_mov_b64 exec, 0\n\t"
    "s_branch .Lloop_%=\n\t"
    ".Lp2_%=:\n\t"
    "s_mov_b64 exec, -1\n\t"
    "s_mov_b32 s96, 0\n\t"
    "s_mov_b32 s97, %[lb]\n\t"
    "s_mov_b32 s41, 0\n\t"
    "s_movk_i32 s45, 1300\n\t"
    "s_add_u32 s50, %[oR], 0\n\t"
    "buffer_load_dwordx4 v[128:131], %[vwa], %[rs], s50 offen offset:0\n\t"
    "buffer_load_dwordx4 v[132:135], %[vwa], %[rs], s50 offen offset:1024\n\t"
    "s_add_u32 s50, %[oR], 2048\n\t"
    "buffer_load_dwordx4 v[136:139], %[vwa], %[rs], s50 offen offset:0\n\t"
    "buffer_load_dwordx4 v[140:143], %[vwa], %[rs], s50 offen offset:1024\n\t"
    "s_add_u32 s50, %[oR], 4096\n\t"
    "buffer_load_dwordx4 v[144:147], %[vwa], %[rs], s50 offen offset:0\n\t"
    "buffer_load_dwordx4 v[148:151], %[vwa], %[rs], s50 offen offset:1024\n\t"
    "s_add_u32 s50, %[oL], 0\n\t"
    "buffer_load_dwordx4 v[152:155], %[vwa], %[rs], s50 offen offset:0\n\t"
    "buffer_load_dwordx4 v[156:159], %[vwa], %[rs], s50 offen offset:1024\n\t"
    "s_add_u32 s50, %[oL], 2048\n\t"
    "buffer_load_dwordx4 v[160:163], %[vwa], %[rs], s50 offen offset:0\n\t"
    "buffer_load_dwordx4 v[164:167], %[vwa], %[rs], s50 offen offset:1024\n\t"
    "s_add_u32 s50, %[oL], 4096\n\t"
    "buffer_load_dwordx4 v[168:171], %[vwa], %[rs], s50 offen offset:0\n\t"
    "buffer_load_dwordx4 v[172:175], %[vwa], %[rs], s50 offen offset:1024\n\t"
    "s_add_u32 s50, %[oM], 0\n\t"
    "buffer_load_dwordx4 v[176:179], %[vwa], %[rs], s50 offen offset:0\n\t"
    "buffer_load_dwordx4 v[180:183], %[vwa], %[rs], s50 offen offset:1024\n\t"
    "s_add_u32 s50, %[oR], 0\n\t"
    "buffer_load_dwordx4 v[184:187], %[vwb], %[rs], s50 offen offset:0\n\t"
    "buffer_load_dwordx4 v[188:191], %[vwb], %[rs], s50 offen offset:1024\n\t"
    "s_add_u32 s50, %[oR], 2048\n\t"
    "buffer_load_dwordx4 v[192:195], %[vwb], %[rs], s50 offen offset:0\n\t"
    "buffer_load_dwordx4 v[196:199], %[vwb], %[rs], s50 offen offset:1024\n\t"
    "s_add_u32 s50, %[oR], 4096\n\t"
    "buffer_load_dwordx4 v[200:203], %[vwb], %[rs], s50 offen offset:0\n\t"
    "buffer_load_dwordx4 v[204:207], %[vwb], %[rs], s50 offen offset:1024\n\t"
    "s_add_u32 s50, %[oL], 0\n\t"
    "buffer_load_dwordx4 v[208:211], %[vwb], %[rs], s50 offen offset:0\n\t"
    "buffer_load_dwordx4 v[212:215], %[vwb], %[rs], s50 offen offset:1024\n\t"
    "s_add_u32 s50, %[oL], 2048\n\t"
    "buffer_load_dwordx4 v[216:219], %[vwb], %[rs], s50 offen offset:0\n\t"
    "buffer_load_dwordx4 v[220:223], %[vwb], %[rs], s50 offen offset:1024\n\t"
    "s_add_u32 s50, %[oL], 4096\n\t"
    "buffer_load_dwordx4 v[224:227], %[vwb], %[rs], s50 offen offset:0\n\t"
    "buffer_load_dwordx4 v[228:231], %[vwb], %[rs], s50 offen offset:1024\n\t"
    "s_add_u32 s50, %[oM], 0\n\t"
    "buffer_load_dwordx4 v[232:235], %[vwb], %[rs], s50 offen offset:0\n\t"
    "buffer_load_dwordx4 v[236:239], %[vwb], %[rs], s50 offen offset:1024\n\t"
    ".Ltb_%=:\n\t"
    "buffer_load_dword v29, %[vtab], %[rs], 0 offen sc1\n\t"
    "s_waitcnt vmcnt(0)\n\t"
    "v_cmp_eq_u32 vcc, 0, v29\n\t"
    "s_cbranch_vccz .Ltq_%=\n\t"
    "s_sub_u32 %[cap], %[cap], 1\n\t"
    "s_cbranch_scc0 .Ltb_%=\n\t"
    "s_mov_b32 %[cap], 0\n\t"
    ".Ltq_%=:\n\t"
    "v_and_b32 v29, -2, v29\n\t"
    "s_mov_b32 s36, 0\n\t"
    "s_nop 0\n\t"
    "v_readlane_b32 s37, v29, 0\n\t"
    "s_mov_b32 s36, 0\n\t"
    "v_readlane_b32 s37, v29, 0\n\t"
    "s_barrier\n\t"
    "s_add_u32 m0, s97, 45056\n\t"
    "s_nop 0\n\t"
    "buffer_load_dwordx4 %[vg0], %[rs], s96 offen sc1 lds\n\t"
    "s_add_u32 m0, s97, 46080\n\t"
    "s_nop 0\n\t"
    "buffer_load_dwordx4 %[vg1], %[rs], s96 offen sc1 lds\n\t"
    "s_add_u32 s96, s96, 458752\n\t"
    "s_add_u32 s97, s97, 0x4000\n\t"
    "s_add_u32 m0, s97, 45056\n\t"
    "s_nop 0\n\t"
    "buffer_load_dwordx4 %[vg0], %[rs], s96 offen sc1 lds\n\t"
    "s_add_u32 m0, s97, 46080\n\t"
    "s_nop 0\n\t"
    "buffer_load_dwordx4 %[vg1], %[rs], s96 offen sc1 lds\n\t"
    "s_add_u32 s96, s96, 458752\n\t"
    "s_setprio 3\n\t"
    "s_waitcnt vmcnt(0)\n\t"
    "s_barrier\n\t"
    ".Lloop_%=:\n\t"
    "s_add_u32 s38, s37, 0x8000\n\t"
    "s_add_u32 s39, s37, 0x10000\n\t"
    "v_add_u32 v30, s41, %[ll]\n\t"
    "v_mov_b64 v[60:61], 0\n\t"
    "v_mov_b64 v[62:63], 0\n\t"
    "v_mov_b64 v[64:65], 0\n\t"
    "v_mov_b64 v[66:67], 0\n\t"
    "s_xor_b32 s97, s41, 0x8000\n\t"
    "s_add_u32 s97, s97, %[lb]\n\t"
    "s_add_u32 s41, s41, 0x4000\n\t"
    "s_and_b32 s41, s41, 0xffff\n\t"
    "s_setprio 3\n\t"
    "s_barrier\n\t"
    ".Lim_%=:\n\t"
    "buffer_load_dwordx4 v[72:75], %[vm0], %[rs], s37 offen sc1\n\t"
    "s_nop 7\n\t"
    "buffer_load_dwordx4 v[76:79], %[vm1], %[rs], s37 offen sc1\n\t"
    "ds_read_b128 v[100:103], v30 offset:36864\n\t"
    "s_waitcnt vmcnt(1) lgkmcnt(0)\n\t"
    "ds_write_b128 %[lf], v[72:75] offset:5120\n\t"
    "v_smfmac_f32_16x16x64_bf16 v[60:63], v[72:75], v[168:175], %[idx]\n\t"
    "v_max3_u32 v24, v72, v73, v74\n\t"
    "v_smfmac_f32_16x16x64_bf16 v[64:67], v[72:75], v[224:231], %[idx]\n\t"
    "s_waitcnt vmcnt(0)\n\t"
    "ds_write_b128 %[lf], v[76:79] offset:2048\n\t"
    "v_max3_u32 v28, v76, v77, v78\n\t"
    "v_max3_u32 v24, v24, v75, v79\n\t"
    "v_smfmac_f32_16x16x64_bf16 v[60:63], v[76:79], v[144:151], %[idx]\n\t"
    "v_max_u32 v28, v28, v24\n\t"
    "v_cmp_eq_u32 vcc, -1, v28\n\t"
    "v_smfmac_f32_16x16x64_bf16 v[64:67], v[76:79], v[200:207], %[idx]\n\t"
    "s_cbranch_vccnz .Lfm_%=\n\t"
    ".Lgd_%=:\n\t"
    "s_waitcnt lgkmcnt(0)\n\t"
    "s_barrier\n\t"
    "s_nop 4\n\t"
    "ds_read_b128 v[40:43], %[lf] offset:0\n\t"
    "ds_read_b128 v[44:47], %[lf] offset:1024\n\t"
    "ds_read_b128 v[48:51], %[lf] offset:3072\n\t"
    "ds_read_b128 v[52:55], %[lf] offset:4096\n\t"
    "ds_read_b128 v[56:59], %[lf] offset:6144\n\t"
    "s_waitcnt lgkmcnt(4)\n\t"
    "v_smfmac_f32_16x16x64_bf16 v[60:63], v[40:43], v[128:135], %[idx]\n\t"
    "v_smfmac_f32_16x16x64_bf16 v[64:67], v[40:43], v[184:191], %[idx]\n\t"
    "s_waitcnt lgkmcnt(3)\n\t"
    "v_smfmac_f32_16x16x64_bf16 v[60:63], v[44:47], v[136:143], %[idx]\n\t"
    "v_smfmac_f32_16x16x64_bf16 v[64:67], v[44:47], v[192:199], %[idx]\n\t"
    "s_waitcnt lgkmcnt(2)\n\t"
    "v_smfmac_f32_16x16x64_bf16 v[60:63], v[48:51], v[152:159], %[idx]\n\t"
    "s_waitcnt lgkmcnt(1)\n\t"
    "v_smfmac_f32_16x16x64_bf16 v[60:63], v[52:55], v[160:167], %[idx]\n\t"
    "s_waitcnt lgkmcnt(0)\n\t"
    "v_smfmac_f32_16x16x64_bf16 v[60:63], v[56:59], v[176:183], %[idx]\n\t"
    "v_smfmac_f32_16x16x64_bf16 v[64:67], v[48:51], v[208:215], %[idx]\n\t"
    "v_smfmac_f32_16x16x64_bf16 v[64:67], v[52:55], v[216:223], %[idx]\n\t"
    "v_smfmac_f32_16x16x64_bf16 v[64:67], v[56:59], v[232:239], %[idx]\n\t"
    "s_nop 4\n\t"
    "v_add_f32 v60, v60, v62\n\t"
    "v_add_f32 v61, v61, v63\n\t"
    "v_cvt_pk_bf16_f32 v24, v60, v61\n\t"
    "v_add_f32 v64, v64, v66\n\t"
    "v_add_f32 v65, v65, v67\n\t"
    "v_cvt_pk_bf16_f32 v25, v64, v65\n\t"
    "ds_write_b64 %[limg], v[24:25]\n\t"
    "s_waitcnt lgkmcnt(0)\n\t"
    "s_barrier\n\t"
    "s_add_u32 s36, s36, 1\n\t"
    "s_cmp_eq_u32 s36, 64\n\t"
    "s_cbranch_scc1 .Lend_%=\n\t"
    "s_barrier\n\t"
    "buffer_load_dwordx4 v[72:75], %[vl], %[rs], s39 offen sc1\n\t"
    "s_setprio 3\n\t"
    "s_nop 3\n\t"
    "s_nop 15\n\t"
    "s_nop 15\n\t"
    "s_nop 15\n\t"
    "buffer_load_dwordx4 v[76:79], %[vl], %[rs], s38 offen sc1\n\t"
    "s_barrier\n\t"
    "ds_read_b64 v[96:97], %[ll] offset:12288\n\t"
    "v_readlane_b32 s37, v29, s36\n\t"
    "s_add_u32 m0, s97, 45056\n\t"
    "s_nop 0\n\t"
    "buffer_load_dwordx4 %[vg0], %[rs], s96 offen sc1 lds\n\t"
    "s_add_u32 m0, s97, 46080\n\t"
    "s_nop 0\n\t"
    "buffer_load_dwordx4 %[vg1], %[rs], s96 offen sc1 lds\n\t"
    "s_add_u32 s96, s96, 458752\n\t"
    "s_waitcnt vmcnt(3) lgkmcnt(0)\n\t"
    ".Lme_%=:\n\t"
    "v_lshrrev_b32 v26, 4, v73\n\t"
    "v_bfi_b32 v26, s42, v72, v26\n\t"
    "v_perm_b32 v24, v26, v26, s44\n\t"
    "v_perm_b32 v25, v26, v26, s44\n\t"
    "v_perm_b32 v39, v26, v26, s44\n\t"
    "v_max3_u32 v28, v72, v73, v74\n\t"
    "v_permlane16_swap_b32 v24, v25\n\t"
    "v_max_f32 v26, v24, v25\n\t"
    "v_max_f32 v27, v24, v25\n\t"
    "v_cmp_u_f32 vcc, v28, v75\n\t"
    "s_nop 0\n\t"
    "v_permlane32_swap_b32 v26, v27\n\t"
    "s_cbranch_vccnz .Lfe_%=\n\t"
    ".Leg_%=:\n\t"
    "v_max_f32 v31, v26, v27\n\t"
    "v_sub_f32 v24, v39, v31\n\t"
    "v_add_f32 v24, 0x42c80000, v24\n\t"
    "v_cvt_u32_f32 v24, v24\n\t"
    "v_and_b32 v72, s43, v72\n\t"
    "v_mul_u32_u24 v24, 0x800080, v24\n\t"
    "v_and_b32 v73, s43, v73\n\t"
    "v_add_u32 v74, v74, v24\n\t"
    "v_add_u32 v75, v75, v24\n\t"
    "v_add_u32 v72, v72, v24\n\t"
    "v_add_u32 v73, v73, v24\n\t"
    "s_nop 1\n\t"
    "v_mfma_f32_16x16x32_bf16 v[40:43], v[100:103], v[72:75], 0\n\t"
    "v_mfma_f32_16x16x32_bf16 v[44:47], v[104:107], v[72:75], 0\n\t"
    "s_waitcnt vmcnt(2)\n\t"
    ".Lms_%=:\n\t"
    "v_mfma_f32_16x16x32_bf16 v[48:51], %[sel], v[76:79], 0\n\t"
    "v_max3_u32 v28, v76, v77, v78\n\t"
    "v_cmp_u_f32 vcc, v28, v79\n\t"
    "s_nop 1\n\t"
    "s_cbranch_vccnz .Lfs_%=\n\t"
    ".Lsg_%=:\n\t"
    "s_nop 0\n\t"
    "v_cndmask_b32_e64 v40, v40, v42, s[46:47]\n\t"
    "v_cndmask_b32_e64 v41, v41, v43, s[46:47]\n\t"
    "v_max3_f32 v24, v48, v49, v50\n\t"
    "v_max3_f32 v25, v24, v51, v31\n\t"
    "v_max3_f32 v24, v24, v51, v31\n\t"
    "s_nop 1\n\t"
    "v_permlane32_swap_b32 v24, v25\n\t"
    "v_max_f32 v32, v24, v25\n\t"
    "v_sub_f32 v48, v48, v32\n\t"
    "v_sub_f32 v49, v49, v32\n\t"
    "v_exp_f32 v48, v48\n\t"
    "v_exp_f32 v49, v49\n\t"
    "v_sub_f32 v36, v31, v32\n\t"
    "v_cvt_pk_bf16_f32 v33, v48, v49\n\t"
    "v_and_b32 v34, v33, v37\n\t"
    "v_and_b32 v35, v33, v38\n\t"
    "s_nop 1\n\t"
    "v_mfma_f32_16x16x16_bf16 v[52:55], v[96:97], v[34:35], 0\n\t"
    "v_exp_f32 v36, v36\n\t"
    "s_nop 6\n\t"
    "v_fma_f32 v24, v44, v36, v54\n\t"
    "v_rcp_f32 v24, v24\n\t"
    "v_fma_f32 v52, v40, v36, v52\n\t"
    "v_fma_f32 v53, v41, v36, v53\n\t"
    "v_mul_f32 v52, v52, v24\n\t"
    "v_mul_f32 v53, v53, v24\n\t"
    "s_nop 0\n\t"
    "v_add_f32_dpp v52, v52, v52 row_ror:8 row_mask:0xf bank_mask:0xf\n\t"
    "v_add_f32_dpp v53, v53, v53 row_ror:8 row_mask:0xf bank_mask:0xf\n\t"
    "v_cvt_pk_bf16_f32 v24, v52, v53\n\t"
    "ds_write_b32 %[lred], v24\n\t"
    "s_waitcnt lgkmcnt(0)\n\t"
    "s_barrier\n\t"
    "s_cbranch_execnz .Lloop_%=\n\t"
    "s_branch .Lp2_%=\n\t"
    "s_nop 7\n\t"
    ".Lfm_%=:\n\t"
    "ds_write_b32 v80, v81 offset:14336\n\t"
    "s_sub_u32 %[cap], %[cap], 1\n\t"
    "s_cbranch_scc1 .Ldm_%=\n\t"
    "s_nop 5\n\t"
    "v_mov_b64 v[60:61], 0\n\t"
    "v_mov_b64 v[62:63], 0\n\t"
    "v_mov_b64 v[64:65], 0\n\t"
    "v_mov_b64 v[66:67], 0\n\t"
    "s_branch .Lim_%=\n\t"
    ".Ldm_%=:\n\t"
    "s_mov_b32 %[cap], 0\n\t"
    "s_branch .Lgd_%=\n\t"
    ".Lfe_%=:\n\t"
    "v_max3_u32 v28, v72, v73, v74\n\t"
    "v_max_u32 v28, v28, v75\n\t"
    "v_cmp_eq_u32 vcc, -1, v28\n\t"
    "s_nop 0\n\t"
    "s_cbranch_vccz .Leg_%=\n\t"
    "ds_write_b32 v80, v81 offset:14336\n\t"
    "s_sub_u32 %[cap], %[cap], 1\n\t"
    "s_cbranch_scc1 .Lde_%=\n\t"
    "buffer_load_dwordx4 v[72:75], %[vl], %[rs], s39 offen sc1\n\t"
    "buffer_load_dwordx4 v[76:79], %[vl], %[rs], s38 offen sc1\n\t"
    "s_waitcnt vmcnt(0)\n\t"
    "s_branch .Lme_%=\n\t"
    ".Lde_%=:\n\t"
    "s_mov_b32 %[cap], 0\n\t"
    "s_branch .Leg_%=\n\t"
    ".Lfs_%=:\n\t"
    "v_max3_u32 v28, v76, v77, v78\n\t"
    "v_max_u32 v28, v28, v79\n\t"
    "v_cmp_eq_u32 vcc, -1, v28\n\t"
    "s_nop 0\n\t"
    "s_cbranch_vccz .Lsg_%=\n\t"
    "ds_write_b32 v80, v81 offset:14336\n\t"
    "s_sub_u32 %[cap], %[cap], 1\n\t"
    "s_cbranch_scc1 .Lds_%=\n\t"
    "buffer_load_dwordx4 v[76:79], %[vl], %[rs], s38 offen sc1\n\t"
    "s_waitcnt vmcnt(0)\n\t"
    "s_branch .Lms_%=\n\t"
    ".Lds_%=:\n\t"
    "s_mov_b32 %[cap], 0\n\t"
    "s_branch .Lsg_%=\n\t"
    ".Lend_%=:\n\t"
    "s_barrier\n\t"
    "s_barrier\n\t"
    "s_waitcnt vmcnt(0)\n\t"
    "s_setprio 0\n\t"
    "s_mov_b32 m0, s98\n\t"
    MAIN_OPS);
  if (K == 3) asm volatile(
    "s_mov_b32 s98, m0\n\t"
    "s_getpc_b64 s[48:49]\n\t"
    ".Lwp_%=:\n\t"
    "v_mbcnt_lo_u32_b32 v24, -1, 0\n\t"
    "v_mbcnt_hi_u32_b32 v24, -1, v24\n\t"
    "v_lshlrev_b32 v24, 7, v24\n\t"
    "v_min_u32 v24, .Lend_%=-.Lwp_%=, v24\n\t"
    "global_load_dword v24, v24, s[48:49]\n\t"
    "s_mov_b32 s46, 0xff00ff00\n\t"
    "s_mov_b32 s47, 0xff00ff00\n\t"
    "s_mov_b32 s41, 0\n\t"
    "s_mov_b32 s42, 0xf000f000\n\t"
    "s_mov_b32 s43, 0x0fff0fff\n\t"
    "s_mov_b32 s44, 0x03010c0c\n\t"
    "v_mov_b32 v104, 0x3f803f80\n\t"
    "v_mov_b32 v105, 0x3f803f80\n\t"
    "v_mov_b32 v106, 0x3f803f80\n\t"
    "v_mov_b32 v107, 0x3f803f80\n\t"
    "s_mov_b32 s50, 512\n\t"
    "v_mov_b32 v81, s50\n\t"
    "v_mov_b32 v80, %[lb]\n\t"
    "v_cndmask_b32_e64 v37, -1, 0, s[46:47]\n\t"
    "v_cndmask_b32_e64 v38, 0, -1, s[46:47]\n\t"
    "s_mov_b32 s99, 0x030c020c\n\t"
    "s_mov_b32 s40, 0xbb800000\n\t"
    "v_mov_b32 v109, 0x32003200\n\t"
    "s_mov_b32 s45, 0\n\t"
    "s_barrier\n\t"
    "s_barrier\n\t"
    "v_mov_b32 v82, 0\n\t"
    "s_mov_b32 s96, 0\n\t"
    "s_mov_b32 s97, %[lb]\n\t"
    "s_mov_b32 s41, 0\n\t"
    "s_mov_b32 s36, 0\n\t"
    "s_mov_b32 s37, 0\n\t"
    "s_mov_b64 exec, 0\n\t"
    "s_branch .Lloop_%=\n\t"
    ".Lp2_%=:\n\t"
    "s_mov_b64 exec, -1\n\t"
    "s_mov_b32 s96, 0\n\t"
    "s_mov_b32 s97, %[lb]\n\t"
    "s_mov_b32 s41, 0\n\t"
    "s_movk_i32 s45, 1300\n\t"
    "s_add_u32 s50, %[oR], 0\n\t"
    "buffer_load_dwordx4 v[128:131], %[vwa], %[rs], s50 offen offset:0\n\t"
    "buffer_load_dwordx4 v[132:135], %[vwa], %[rs], s50 offen offset:1024\n\t"
    "s_add_u32 s50, %[oR], 2048\n\t"
    "buffer_load_dwordx4 v[136:139], %[vwa], %[rs], s50 offen offset:0\n\t"
    "buffer_load_dwordx4 v[140:143], %[vwa], %[rs], s50 offen offset:1024\n\t"
    "s_add_u32 s50, %[oR], 4096\n\t"
    "buffer_load_dwordx4 v[144:147], %[vwa], %[rs], s50 offen offset:0\n\t"
    "buffer_load_dwordx4 v[148:151], %[vwa], %[rs], s50 offen offset:1024\n\t"
    "s_add_u32 s50, %[oL], 0\n\t"
    "buffer_load_dwordx4 v[152:155], %[vwa], %[rs], s50 offen offset:0\n\t"
    "buffer_load_dwordx4 v[156:159], %[vwa], %[rs], s50 offen offset:1024\n\t"
    "s_add_u32 s50, %[oL], 2048\n\t"
    "buffer_load_dwordx4 v[160:163], %[vwa], %[rs], s50 offen offset:0\n\t"
    "buffer_load_dwordx4 v[164:167], %[vwa], %[rs], s50 offen offset:1024\n\t"
    "s_add_u32 s50, %[oL], 4096\n\t"
    "buffer_load_dwordx4 v[168:171], %[vwa], %[rs], s50 offen offset:0\n\t"
    "buffer_load_dwordx4 v[172:175], %[vwa], %[rs], s50 offen offset:1024\n\t"
    "s_add_u32 s50, %[oM], 0\n\t"
    "buffer_load_dwordx4 v[176:179], %[vwa], %[rs], s50 offen offset:0\n\t"
    "buffer_load_dwordx4 v[180:183], %[vwa], %[rs], s50 offen offset:1024\n\t"
    ".Ltb_%=:\n\t"
    "buffer_load_dword v29, %[vtab], %[rs], 0 offen sc1\n\t"
    "s_waitcnt vmcnt(0)\n\t"
    "v_cmp_eq_u32 vcc, 0, v29\n\t"
    "s_cbranch_vccz .Ltq_%=\n\t"
    "s_sub_u32 %[cap], %[cap], 1\n\t"
    "s_cbranch_scc0 .Ltb_%=\n\t"
    "s_mov_b32 %[cap], 0\n\t"
    ".Ltq_%=:\n\t"
    "v_and_b32 v29, -2, v29\n\t"
    "s_mov_b32 s36, 0\n\t"
    "s_nop 0\n\t"
    "v_readlane_b32 s37, v29, 0\n\t"
    "s_mov_b32 s36, 0\n\t"
    "v_readlane_b32 s37, v29, 0\n\t"
    "s_barrier\n\t"
    "s_add_u32 m0, s97, 47104\n\t"
    "s_nop 0\n\t"
    "buffer_load_dwordx4 %[vg0], %[rs], s96 offen sc1 lds\n\t"
    "s_add_u32 s96, s96, 458752\n\t"
    "s_add_u32 s97, s97, 0x4000\n\t"
    "s_add_u32 m0, s97, 47104\n\t"
    "s_nop 0\n\t"
    "buffer_load_dwordx4 %[vg0], %[rs], s96 offen sc1 lds\n\t"
    "s_add_u32 s96, s96, 458752\n\t"
    "s_setprio 3\n\t"
    "s_waitcnt vmcnt(0)\n\t"
    "s_barrier\n\t"
    ".Lloop_%=:\n\t"
    "s_add_u32 s38, s37, 0x8000\n\t"
    "s_add_u32 s39, s37, 0x10000\n\t"
    "v_add_u32 v30, s41, %[ll]\n\t"
    "v_mov_b64 v[60:61], 0\n\t"
    "v_mov_b64 v[62:63], 0\n\t"
    "v_mov_b64 v[64:65], 0\n\t"
    "v_mov_b64 v[66:67], 0\n\t"
    "s_xor_b32 s97, s41, 0x8000\n\t"
    "s_add_u32 s97, s97, %[lb]\n\t"
    "s_add_u32 s41, s41, 0x4000\n\t"
    "s_and_b32 s41, s41, 0xffff\n\t"
    "s_setprio 3\n\t"
    "s_barrier\n\t"
    ".Lim_%=:\n\t"
    "buffer_load_dwordx4 v[72:75], %[vm0], %[rs], s37 offen sc1\n\t"
    "s_nop 7\n\t"
    "buffer_load_dwordx4 v[76:79], %[vm1], %[rs], s37 offen sc1\n\t"
    "ds_read_b128 v[100:103], v30 offset:38912\n\t"
    "ds_read_b128 v[184:187], v30 offset:40960\n\t"
    "ds_read_b128 v[188:191], v30 offset:41984\n\t"
    "ds_read_b128 v[192:195], v30 offset:43008\n\t"
    "ds_read_b128 v[196:199], v30 offset:44032\n\t"
    "ds_read_b128 v[200:203], v30 offset:45056\n\t"
    "ds_read_b128 v[204:207], v30 offset:46080\n\t"
    "ds_read_b128 v[208:211], v30 offset:47104\n\t"
    "s_waitcnt vmcnt(1) lgkmcnt(0)\n\t"
    "v_smfmac_f32_16x16x64_bf16 v[60:63], v[72:75], v[152:159], %[idx]\n\t"
    "v_max3_u32 v24, v72, v73, v74\n\t"
    "v_mfma_f32_16x16x32_bf16 v[64:67], v[72:75], v[196:199], v[64:67]\n\t"
    "s_waitcnt vmcnt(0)\n\t"
    "ds_write_b128 %[lf], v[76:79] offset:6144\n\t"
    "v_max3_u32 v28, v76, v77, v78\n\t"
    "v_max3_u32 v24, v24, v75, v79\n\t"
    "v_smfmac_f32_16x16x64_bf16 v[60:63], v[76:79], v[176:183], %[idx]\n\t"
    "v_max_u32 v28, v28, v24\n\t"
    "v_cmp_eq_u32 vcc, -1, v28\n\t"
    "v_mfma_f32_16x16x32_bf16 v[64:67], v[76:79], v[208:211], v[64:67]\n\t"
    "s_cbranch_vccnz .Lfm_%=\n\t"
    ".Lgd_%=:\n\t"
    "s_waitcnt lgkmcnt(0)\n\t"
    "s_barrier\n\t"
    "ds_read_b128 v[40:43], %[lf] offset:0\n\t"
    "ds_read_b128 v[44:47], %[lf] offset:1024\n\t"
    "ds_read_b128 v[48:51], %[lf] offset:2048\n\t"
    "ds_read_b128 v[52:55], %[lf] offset:4096\n\t"
    "ds_read_b128 v[56:59], %[lf] offset:5120\n\t"
    "s_waitcnt lgkmcnt(4)\n\t"
    "v_mfma_f32_16x16x32_bf16 v[64:67], v[40:43], v[184:187], v[64:67]\n\t"
    "v_smfmac_f32_16x16x64_bf16 v[60:63], v[40:43], v[128:135], %[idx]\n\t"
    "s_waitcnt lgkmcnt(3)\n\t"
    "v_mfma_f32_16x16x32_bf16 v[64:67], v[44:47], v[188:191], v[64:67]\n\t"
    "v_smfmac_f32_16x16x64_bf16 v[60:63], v[44:47], v[136:143], %[idx]\n\t"
    "s_waitcnt lgkmcnt(2)\n\t"
    "v_mfma_f32_16x16x32_bf16 v[64:67], v[48:51], v[192:195], v[64:67]\n\t"
    "v_smfmac_f32_16x16x64_bf16 v[60:63], v[48:51], v[144:151], %[idx]\n\t"
    "s_waitcnt lgkmcnt(1)\n\t"
    "v_mfma_f32_16x16x32_bf16 v[64:67], v[52:55], v[200:203], v[64:67]\n\t"
    "v_smfmac_f32_16x16x64_bf16 v[60:63], v[52:55], v[160:167], %[idx]\n\t"
    "s_waitcnt lgkmcnt(0)\n\t"
    "v_mfma_f32_16x16x32_bf16 v[64:67], v[56:59], v[204:207], v[64:67]\n\t"
    "v_smfmac_f32_16x16x64_bf16 v[60:63], v[56:59], v[168:175], %[idx]\n\t"
    "v_mov_b32 v25, 0\n\t"
    "s_nop 5\n\t"
    "v_add_f32_dpp v26, v66, v64 row_ror:8 row_mask:0xf bank_mask:0x3\n\t"
    "v_add_f32 v60, v60, v62\n\t"
    "v_add_f32 v61, v61, v63\n\t"
    "v_add_f32_dpp v26, v65, v67 row_ror:8 row_mask:0xf bank_mask:0xc\n\t"
    "v_cvt_pk_bf16_f32 v24, v60, v61\n\t"
    "ds_write_b64 %[limg], v[24:25]\n\t"
    "v_max_f32_dpp v27, v26, v26 quad_perm:[1,0,3,2] row_mask:0xf bank_mask:0xf\n\t"
    "s_nop 1\n\t"
    "v_max_f32_dpp v27, v27, v27 quad_perm:[2,3,0,1] row_mask:0xf bank_mask:0xf\n\t"
    "s_nop 1\n\t"
    "v_max_f32_dpp v27, v27, v27 row_half_mirror row_mask:0xf bank_mask:0xf\n\t"
    "v_fma_f32 v27, |v27|, s40, v27\n\t"
    "v_ceil_f32 v27, v27\n\t"
    "v_cvt_pk_bf16_f32 v27, 0, v27\n\t"
    "v_sub_f32 v26, v26, v27\n\t"
    "v_min_f32 v26, 2.0, v26\n\t"
    "v_exp_f32 v26, v26\n\t"
    "v_perm_b32 v27, v27, v27, s99\n\t"
    "v_lshlrev_b32 v27, %[c0], v27\n\t"
    "v_mov_b32_dpp v28, v26 quad_perm:[1,0,3,2] row_mask:0xf bank_mask:0xf\n\t"
    "v_cvt_pk_bf16_f32 v26, v26, v28\n\t"
    "v_pk_sub_u16 v26, v26, v109 clamp\n\t"
    "v_and_or_b32 v26, v27, %[c1], v26\n\t"
    "buffer_store_dword v26, %[vpe], %[rs], s39 offen sc1\n\t"
    "s_waitcnt lgkmcnt(0)\n\t"
    "s_barrier\n\t"
    "s_add_u32 s36, s36, 1\n\t"
    "s_cmp_eq_u32 s36, 64\n\t"
    "s_cbranch_scc1 .Lend_%=\n\t"
    "s_barrier\n\t"
    "buffer_load_dwordx4 v[72:75], %[vl], %[rs], s39 offen sc1\n\t"
    "s_setprio 3\n\t"
    "s_nop 3\n\t"
    "s_nop 15\n\t"
    "s_nop 15\n\t"
    "s_nop 15\n\t"
    "buffer_load_dwordx4 v[76:79], %[vl], %[rs], s38 offen sc1\n\t"
    "s_barrier\n\t"
    "ds_read_b64 v[96:97], %[ll] offset:13312\n\t"
    "v_readlane_b32 s37, v29, s36\n\t"
    "s_add_u32 m0, s97, 47104\n\t"
    "s_nop 0\n\t"
    "buffer_load_dwordx4 %[vg0], %[rs], s96 offen sc1 lds\n\t"
    "s_add_u32 s96, s96, 458752\n\t"
    "s_waitcnt vmcnt(2) lgkmcnt(0)\n\t"
    ".Lme_%=:\n\t"
    "v_lshrrev_b32 v26, 4, v73\n\t"
    "v_bfi_b32 v26, s42, v72, v26\n\t"
    "v_perm_b32 v24, v26, v26, s44\n\t"
    "v_perm_b32 v25, v26, v26, s44\n\t"
    "v_perm_b32 v39, v26, v26, s44\n\t"
    "v_max3_u32 v28, v72, v73, v74\n\t"
    "v_permlane16_swap_b32 v24, v25\n\t"
    "v_max_f32 v26, v24, v25\n\t"
    "v_max_f32 v27, v24, v25\n\t"
    "v_cmp_u_f32 vcc, v28, v75\n\t"
    "s_nop 0\n\t"
    "v_permlane32_swap_b32 v26, v27\n\t"
    "s_cbranch_vccnz .Lfe_%=\n\t"
    ".Leg_%=:\n\t"
    "v_max_f32 v31, v26, v27\n\t"
    "v_sub_f32 v24, v39, v31\n\t"
    "v_add_f32 v24, 0x42c80000, v24\n\t"
    "v_cvt_u32_f32 v24, v24\n\t"
    "v_and_b32 v72, s43, v72\n\t"
    "v_mul_u32_u24 v24, 0x800080, v24\n\t"
    "v_and_b32 v73, s43, v73\n\t"
    "v_add_u32 v74, v74, v24\n\t"
    "v_add_u32 v75, v75, v24\n\t"
    "v_add_u32 v72, v72, v24\n\t"
    "v_add_u32 v73, v73, v24\n\t"
    "s_nop 1\n\t"
    "v_mfma_f32_16x16x32_bf16 v[40:43], v[100:103], v[72:75], 0\n\t"
    "v_mfma_f32_16x16x32_bf16 v[44:47], v[104:107], v[72:75], 0\n\t"
    "s_waitcnt vmcnt(1)\n\t"
    ".Lms_%=:\n\t"
    "v_mfma_f32_16x16x32_bf16 v[48:51], %[sel], v[76:79], 0\n\t"
    "v_max3_u32 v28, v76, v77, v78\n\t"
    "v_cmp_u_f32 vcc, v28, v79\n\t"
    "s_nop 1\n\t"
    "s_cbranch_vccnz .Lfs_%=\n\t"
    ".Lsg_%=:\n\t"
    "s_nop 0\n\t"
    "v_cndmask_b32_e64 v40, v40, v42, s[46:47]\n\t"
    "v_cndmask_b32_e64 v41, v41, v43, s[46:47]\n\t"
    "v_max3_f32 v24, v48, v49, v50\n\t"
    "v_max3_f32 v25, v24, v51, v31\n\t"
    "v_max3_f32 v24, v24, v51, v31\n\t"
    "s_nop 1\n\t"
    "v_permlane32_swap_b32 v24, v25\n\t"
    "v_max_f32 v32, v24, v25\n\t"
    "v_sub_f32 v48, v48, v32\n\t"
    "v_sub_f32 v49, v49, v32\n\t"
    "v_exp_f32 v48, v48\n\t"
    "v_exp_f32 v49, v49\n\t"
    "v_sub_f32 v36, v31, v32\n\t"
    "v_cvt_pk_bf16_f32 v33, v48, v49\n\t"
    "v_and_b32 v34, v33, v37\n\t"
    "v_and_b32 v35, v33, v38\n\t"
    "s_nop 1\n\t"
    "v_mfma_f32_16x16x16_bf16 v[52:55], v[96:97], v[34:35], 0\n\t"
    "v_exp_f32 v36, v36\n\t"
    "s_nop 6\n\t"
    "v_fma_f32 v24, v44, v36, v54\n\t"
    "v_rcp_f32 v24, v24\n\t"
    "v_fma_f32 v52, v40, v36, v52\n\t"
    "v_fma_f32 v53, v41, v36, v53\n\t"
    "v_mul_f32 v52, v52, v24\n\t"
    "v_mul_f32 v53, v53, v24\n\t"
    "s_nop 0\n\t"
    "v_add_f32_dpp v52, v52, v52 row_ror:8 row_mask:0xf bank_mask:0xf\n\t"
    "v_add_f32_dpp v53, v53, v53 row_ror:8 row_mask:0xf bank_mask:0xf\n\t"
    "v_cvt_pk_bf16_f32 v24, v52, v53\n\t"
    "ds_write_b32 %[lred], v24\n\t"
    "s_waitcnt lgkmcnt(0)\n\t"
    "s_barrier\n\t"
    "s_cbranch_execnz .Lloop_%=\n\t"
    "s_branch .Lp2_%=\n\t"
    "s_nop 7\n\t"
    ".Lfm_%=:\n\t"
    "ds_write_b32 v80, v81 offset:14336\n\t"
    "s_sub_u32 %[cap], %[cap], 1\n\t"
    "s_cbranch_scc1 .Ldm_%=\n\t"
    "s_nop 5\n\t"
    "v_mov_b64 v[60:61], 0\n\t"
    "v_mov_b64 v[62:63], 0\n\t"
    "v_mov_b64 v[64:65], 0\n\t"
    "v_mov_b64 v[66:67], 0\n\t"
    "s_branch .Lim_%=\n\t"
    ".Ldm_%=:\n\t"
    "s_mov_b32 %[cap], 0\n\t"
    "s_branch .Lgd_%=\n\t"
    ".Lfe_%=:\n\t"
    "v_max3_u32 v28, v72, v73, v74\n\t"
    "v_max_u32 v28, v28, v75\n\t"
    "v_cmp_eq_u32 vcc, -1, v28\n\t"
    "s_nop 0\n\t"
    "s_cbranch_vccz .Leg_%=\n\t"
    "ds_write_b32 v80, v81 offset:14336\n\t"
    "s_sub_u32 %[cap], %[cap], 1\n\t"
    "s_cbranch_scc1 .Lde_%=\n\t"
    "buffer_load_dwordx4 v[72:75], %[vl], %[rs], s39 offen sc1\n\t"
    "buffer_load_dwordx4 v[76:79], %[vl], %[rs], s38 offen sc1\n\t"
    "s_waitcnt vmcnt(0)\n\t"
    "s_branch .Lme_%=\n\t"
    ".Lde_%=:\n\t"
    "s_mov_b32 %[cap], 0\n\t"
    "s_branch .Leg_%=\n\t"
    ".Lfs_%=:\n\t"
    "v_max3_u32 v28, v76, v77, v78\n\t"
    "v_max_u32 v28, v28, v79\n\t"
    "v_cmp_eq_u32 vcc, -1, v28\n\t"
    "s_nop 0\n\t"
    "s_cbranch_vccz .Lsg_%=\n\t"
    "ds_write_b32 v80, v81 offset:14336\n\t"
    "s_sub_u32 %[cap], %[cap], 1\n\t"
    "s_cbranch_scc1 .Lds_%=\n\t"
    "buffer_load_dwordx4 v[76:79], %[vl], %[rs], s38 offen sc1\n\t"
    "s_waitcnt vmcnt(0)\n\t"
    "s_branch .Lms_%=\n\t"
    ".Lds_%=:\n\t"
    "s_mov_b32 %[cap], 0\n\t"
    "s_branch .Lsg_%=\n\t"
    ".Lend_%=:\n\t"
    "s_barrier\n\t"
    "s_barrier\n\t"
    "s_waitcnt vmcnt(0)\n\t"
    "s_setprio 0\n\t"
    "s_mov_b32 m0, s98\n\t"
    MAIN_OPS);
}
template <int K>
DI void chain_help(const Args& a, rsrc_t ws, u32 xb, u32 slot, char* lds, u32 lb) {
  const u32 lane = threadIdx.x & 63, r = lane & 15, g = lane >> 4;
  const u32 h = 8 * xb + (slot >> 2), j = slot & 3, q = 32 * xb + slot, cq = slot < 28 ? 28 * xb + slot : 0, oo = slot < 28 ? 0u : OOB;
  const u32 half = (r >> 1) & 1, rm = 2 * (r >> 2) + (r & 1), hh = r >> 3, i8 = r & 7;
  const u32 kR = 4 - 4 * xb, kL = 4 * xb;
  auto slotof = [&](u32 kb) { return kb == 3 ? 6u : (kb >> 2) == xb ? 3 + (kb & 3) : (kb & 3); };
  auto mq = [&](u32 m) { u32 c = 8 * m + 4 * hh + g; c = 14 * j + (c < 14 ? c : 13); return lb + L_MEM + slotof(c >> 3) * 1024 + (c & 7) * 128 + i8 * 16; };
  const v4u sel = score_sel(r);
  const u32 gm = (4 * half + g) * 128 + rm * 16;
  v4u selO;
#pragma unroll
  for (u32 d = 0; d < 4; ++d) selO[d] = (d & 1) == (rm >> 2) ? ((rm & 1) ? 0x3f800000u : 0x3f80u) : 0u;
  const u32 a0 = K == 0 ? mq(0) : K == 1 ? lb + L_RED + (g * 8 + i8) * 16 : K == 2 ? (lane ? OOB : F_ZPROG + q * 128) : (lane | q) ? OOB : 56 * 128;
  const u32 a1 = K == 0 ? mq(1) : K == 1 ? ((rm & 2) ? 0xEEEE : 0x4444) : K == 2 ? F_WPROG + ((lane >= 1 && lane < 5) ? 56 + ((14 * h) >> 2) + lane - 1 : cq) * 128 : 56 * 128;
  const v4u rs = {(u32)(uintptr_t)a.ws, (u32)((uintptr_t)a.ws >> 32), WS_END, 0x00020000u};
  u32 cap = SPIN_CAP;
#define HELP_OPS : [cap] "+&s"(cap) \
  : [rs] "s"(rs), [oR] "s"(kR * 2048), [oL] "s"(kL * 2048), [oM] "s"(3 * 2048), [lb] "s"(lb), \
    [lf] "v"(lb + (2 * g + half) * 128 + rm * 16), [ll] "v"(lb + lane * 16), [idx] "v"(half ? 0xEEEE : 0x4444), \
    [vwa] "v"(OFF_WB + cq * WBSZ + (kL + K) * KS * 1024 + gm + oo), [vwb] "v"(OFF_WB + cq * WBSZ + (kR + K) * KS * 1024 + gm + oo), \
    [vu0] "v"(OFF_U + cq * 8192 + (kL + K) * 1024 + lane * 16 + oo), [vu1] "v"(OFF_U + cq * 8192 + (kR + K) * 1024 + lane * 16 + oo), \
    [vz0] "v"(OFF_Z + cq * 2048 + (kL + K) * 256 + r * 16 + g * 4 + oo), [vz1] "v"(OFF_Z + cq * 2048 + (kR + K) * 256 + r * 16 + g * 4 + oo), \
    [vl] "v"(((2 * (kR + K) + hh) * 4 + g) * 128 + i8 * 16 + oo), [lred] "v"(lb + L_RED + ((4 + K) * 8 + i8) * 16 + g * 4), [sel] "v"(sel), [vtab] "v"(F_TAB + lane * 128), \
    [vpub] "v"((K == 0 ? H_S + q * 128 : cq * 128 + oo) + (hh ? OOB : i8 * 16 + g * 4)), [a0] "v"(a0), [a1] "v"(a1), \
    [a2] "v"(lb + (g * 8 + (r >> 2)) * 32 + (r & 3) * 8), [a3] "v"(OFF_GM + h * 7168 + j * 1792 + gm), \
    [a4] "v"(OFF_GM + h * 7168 + j * 1792 + (gm >= 768 ? OOB : gm + 1024)), [a5] "v"(selO), [pb] "s"(q * STEP), [vp] "v"(lane < 8 ? H_S + lane * 16 : OOB), [vi] "v"(K * 1024 + lane * 16), [vi0] "v"(q ? K * 1024 + lane * 16 : OOB), [vt] "v"((lane << 21) + K * 128), [vtb] "v"(lane ? OOB : F_TAB + q * 128), [ta] "s"(38), [tz] "s"(255 - 4) : CLOBH
  if (K == 0) asm volatile(
    "s_mov_b32 s40, m0\n\t"
    "buffer_load_dword v83, %[vt], %[rs], 0 offen sc1\n\t"
    "s_mov_b32 s46, 0xff00ff00\n\t"
    "s_mov_b32 s47, 0xff00ff00\n\t"
    "s_mov_b32 s41, 0\n\t"
    "s_mov_b32 s42, 0xf000f000\n\t"
    "s_mov_b32 s43, 0x0fff0fff\n\t"
    "s_mov_b32 s44, 0x03010c0c\n\t"
    "v_mov_b32 v252, 0x3f803f80\n\t"
    "v_mov_b32 v253, 0x3f803f80\n\t"
    "v_mov_b32 v254, 0x3f803f80\n\t"
    "v_mov_b32 v255, 0x3f803f80\n\t"
    "s_mov_b32 s50, 512\n\t"
    "v_mov_b32 v113, s50\n\t"
    "v_mov_b32 v112, %[lb]\n\t"
    "v_cndmask_b32_e64 v120, -1, 0, s[46:47]\n\t"
    "v_cndmask_b32_e64 v121, 0, -1, s[46:47]\n\t"
    "s_mov_b32 s64, 0xcccccccc\n\t"
    "s_mov_b32 s65, 0xcccccccc\n\t"
    "v_mov_b32 v44, -1\n\t"
    "v_mov_b32 v45, -1\n\t"
    "v_mov_b32 v46, -1\n\t"
    "v_mov_b32 v47, -1\n\t"
    "s_mov_b32 s51, -1\n\t"
    "s_mov_b32 s36, 0\n\t"
    "s_mov_b32 s37, 0\n\t"
    "s_waitcnt vmcnt(0)\n\t"
    ".Lpr_%=:\n\t"
    "s_lshl_b32 s50, s36, 13\n\t"
    "s_add_u32 s50, s50, s37\n\t"
    "s_add_u32 s50, s50, %[pb]\n\t"
    "s_memtime s[96:97]\n\t"
    "s_waitcnt lgkmcnt(0)\n\t"
    "buffer_store_dwordx4 v[44:47], %[vp], %[rs], s50 offen sc1\n\t"
    "s_waitcnt vmcnt(0)\n\t"
    "s_memtime s[98:99]\n\t"
    "s_waitcnt lgkmcnt(0)\n\t"
    "s_sub_u32 s50, s98, s96\n\t"
    "s_lshl_b32 s50, s50, 2\n\t"
    "s_or_b32 s50, s50, s36\n\t"
    "s_min_u32 s51, s51, s50\n\t"
    "s_add_u32 s36, s36, 1\n\t"
    "s_and_b32 s36, s36, 3\n\t"
    "s_add_u32 s37, s37, 2176\n\t"
    "s_cmp_lt_u32 s37, 8704\n\t"
    "s_cbranch_scc1 .Lpr_%=\n\t"
    "v_mov_b32 v40, s51\n\t"
    "ds_write_b32 v112, v40 offset:10240\n\t"
    "s_waitcnt lgkmcnt(0)\n\t"
    "s_barrier\n\t"
    "ds_read_b128 v[40:43], v112 offset:10240\n\t"
    "s_waitcnt lgkmcnt(0)\n\t"
    "v_min3_u32 v40, v40, v41, v42\n\t"
    "v_min_u32 v40, v40, v43\n\t"
    "s_nop 0\n\t"
    "v_readfirstlane_b32 s51, v40\n\t"
    "s_and_b32 s51, s51, 3\n\t"
    "s_lshl_b32 s51, s51, 13\n\t"
    "s_add_u32 s51, s51, %[pb]\n\t"
    "s_add_u32 s50, s51, 0\n\t"
    "buffer_store_dwordx4 v[44:47], %[vi0], %[rs], s50 offen sc1\n\t"
    "s_add_u32 s50, s51, 4096\n\t"
    "buffer_store_dwordx4 v[44:47], %[vi0], %[rs], s50 offen sc1\n\t"
    "s_add_u32 s50, s51, 32768\n\t"
    "buffer_store_dwordx4 v[44:47], %[vi], %[rs], s50 offen sc1\n\t"
    "s_add_u32 s50, s51, 36864\n\t"
    "buffer_store_dwordx4 v[44:47], %[vi], %[rs], s50 offen sc1\n\t"
    "s_add_u32 s50, s51, 65536\n\t"
    "buffer_store_dwordx4 v[44:47], %[vi], %[rs], s50 offen sc1\n\t"
    "s_add_u32 s50, s51, 69632\n\t"
    "buffer_store_dwordx4 v[44:47], %[vi], %[rs], s50 offen sc1\n\t"
    "s_getpc_b64 s[48:49]\n\t"
    ".Lwp_%=:\n\t"
    "v_mbcnt_lo_u32_b32 v82, -1, 0\n\t"
    "v_mbcnt_hi_u32_b32 v82, -1, v82\n\t"
    "v_lshlrev_b32 v82, 7, v82\n\t"
    "v_min_u32 v82, .Lend_%=-.Lwp_%=, v82\n\t"
    "global_load_dword v82, v82, s[48:49]\n\t"
    "s_or_b32 s51, s51, 1\n\t"
    "v_mov_b32 v123, s51\n\t"
    "s_waitcnt vmcnt(1)\n\t"
    "s_barrier\n\t"
    "buffer_store_dword v123, %[vtb], %[rs], 0 offen sc1\n\t"
    "v_mov_b32 v114, 0\n\t"
    "v_mov_b32 v122, 0\n\t"
    "s_mov_b32 s96, 0\n\t"
    "s_mov_b32 s97, 0\n\t"
    "s_mov_b32 s98, 0\n\t"
    "s_mov_b32 s99, 1\n\t"
    "s_mov_b32 s41, 0\n\t"
    "s_mov_b32 s36, 0\n\t"
    "s_mov_b32 s37, 0\n\t"
    "s_mov_b64 exec, 0\n\t"
    "s_branch .Lloop_%=\n\t"
    ".Lp2_%=:\n\t"
    "s_mov_b64 exec, -1\n\t"
    "s_mov_b32 s96, 0\n\t"
    "s_mov_b32 s97, 0\n\t"
    "s_mov_b32 s98, 0\n\t"
    "s_mov_b32 s99, 1\n\t"
    "s_mov_b32 s41, 0\n\t"
    "s_add_u32 s50, %[oR], 0\n\t"
    "buffer_load_dwordx4 v[128:131], %[vwa], %[rs], s50 offen offset:0\n\t"
    "buffer_load_dwordx4 v[132:135], %[vwa], %[rs], s50 offen offset:1024\n\t"
    "s_add_u32 s50, %[oR], 2048\n\t"
    "buffer_load_dwordx4 v[136:139], %[vwa], %[rs], s50 offen offset:0\n\t"
    "buffer_load_dwordx4 v[140:143], %[vwa], %[rs], s50 offen offset:1024\n\t"
    "s_add_u32 s50, %[oR], 4096\n\t"
    "buffer_load_dwordx4 v[144:147], %[vwa], %[rs], s50 offen offset:0\n\t"
    "buffer_load_dwordx4 v[148:151], %[vwa], %[rs], s50 offen offset:1024\n\t"
    "s_add_u32 s50, %[oL], 0\n\t"
    "buffer_load_dwordx4 v[152:155], %[vwa], %[rs], s50 offen offset:0\n\t"
    "buffer_load_dwordx4 v[156:159], %[vwa], %[rs], s50 offen offset:1024\n\t"
    "s_add_u32 s50, %[oL], 2048\n\t"
    "buffer_load_dwordx4 v[160:163], %[vwa], %[rs], s50 offen offset:0\n\t"
    "buffer_load_dwordx4 v[164:167], %[vwa], %[rs], s50 offen offset:1024\n\t"
    "s_add_u32 s50, %[oL], 4096\n\t"
    "buffer_load_dwordx4 v[168:171], %[vwa], %[rs], s50 offen offset:0\n\t"
    "buffer_load_dwordx4 v[172:175], %[vwa], %[rs], s50 offen offset:1024\n\t"
    "s_add_u32 s50, %[oM], 0\n\t"
    "buffer_load_dwordx4 v[176:179], %[vwa], %[rs], s50 offen offset:0\n\t"
    "buffer_load_dwordx4 v[180:183], %[vwa], %[rs], s50 offen offset:1024\n\t"
    "s_add_u32 s50, %[oR], 0\n\t"
    "buffer_load_dwordx4 v[184:187], %[vwb], %[rs], s50 offen offset:0\n\t"
    "buffer_load_dwordx4 v[188:191], %[vwb], %[rs], s50 offen offset:1024\n\t"
    "s_add_u32 s50, %[oR], 2048\n\t"
    "buffer_load_dwordx4 v[192:195], %[vwb], %[rs], s50 offen offset:0\n\t"
    "buffer_load_dwordx4 v[196:199], %[vwb], %[rs], s50 offen offset:1024\n\t"
    "s_add_u32 s50, %[oR], 4096\n\t"
    "buffer_load_dwordx4 v[200:203], %[vwb], %[rs], s50 offen offset:0\n\t"
    "buffer_load_dwordx4 v[204:207], %[vwb], %[rs], s50 offen offset:1024\n\t"
    "s_add_u32 s50, %[oL], 0\n\t"
    "buffer_load_dwordx4 v[208:211], %[vwb], %[rs], s50 offen offset:0\n\t"
    "buffer_load_dwordx4 v[212:215], %[vwb], %[rs], s50 offen offset:1024\n\t"
    "s_add_u32 s50, %[oL], 2048\n\t"
    "buffer_load_dwordx4 v[216:219], %[vwb], %[rs], s50 offen offset:0\n\t"
    "buffer_load_dwordx4 v[220:223], %[vwb], %[rs], s50 offen offset:1024\n\t"
    "s_add_u32 s50, %[oL], 4096\n\t"
    "buffer_load_dwordx4 v[224:227], %[vwb], %[rs], s50 offen offset:0\n\t"
    "buffer_load_dwordx4 v[228:231], %[vwb], %[rs], s50 offen offset:1024\n\t"
    "s_add_u32 s50, %[oM], 0\n\t"
    "buffer_load_dwordx4 v[232:235], %[vwb], %[rs], s50 offen offset:0\n\t"
    "buffer_load_dwordx4 v[236:239], %[vwb], %[rs], s50 offen offset:1024\n\t"
    ".Ltb_%=:\n\t"
    "buffer_load_dword v123, %[vtab], %[rs], 0 offen sc1\n\t"
    "s_waitcnt vmcnt(0)\n\t"
    "v_cmp_eq_u32 vcc, 0, v123\n\t"
    "s_cbranch_vccz .Ltq_%=\n\t"
    "s_sub_u32 %[cap], %[cap], 1\n\t"
    "s_cbranch_scc0 .Ltb_%=\n\t"
    "s_mov_b32 %[cap], 0\n\t"
    ".Ltq_%=:\n\t"
    "v_and_b32 v123, -2, v123\n\t"
    "s_mov_b32 s36, 0\n\t"
    "s_nop 0\n\t"
    "v_readlane_b32 s37, v123, 0\n\t"
    "s_mov_b32 s36, 0\n\t"
    "v_readlane_b32 s37, v123, 0\n\t"
    "s_barrier\n\t"
    "s_add_u32 s50, s41, %[lb]\n\t"
    "s_add_u32 m0, s50, 32768\n\t"
    "s_nop 0\n\t"
    "buffer_load_dwordx4 %[vu0], %[rs], s98 offen sc1 lds\n\t"
    "s_add_u32 m0, s50, 33792\n\t"
    "s_nop 0\n\t"
    "buffer_load_dwordx4 %[vu1], %[rs], s98 offen sc1 lds\n\t"
    "s_add_u32 s98, s98, 458752\n\t"
    "s_waitcnt vmcnt(0)\n\t"
    "s_barrier\n\t"
    ".Lloop_%=:\n\t"
    "s_nop 15\n\t"
    "s_nop 11\n\t"
    "s_add_u32 s38, s37, 0x8000\n\t"
    "s_add_u32 s39, s37, 0x10000\n\t"
    "v_add_u32 v124, s41, %[ll]\n\t"
    "s_add_u32 s41, s41, 0x4000\n\t"
    "s_and_b32 s41, s41, 0xffff\n\t"
    "ds_read_b128 v[248:251], v124 offset:33792\n\t"
    "v_mov_b64 v[68:69], 0\n\t"
    "v_mov_b64 v[70:71], 0\n\t"
    "v_mov_b64 v[72:73], 0\n\t"
    "v_mov_b64 v[74:75], 0\n\t"
    "s_barrier\n\t"
    "s_nop 15\n\t"
    "s_nop 7\n\t"
    "s_add_u32 s50, s41, %[lb]\n\t"
    "s_add_u32 m0, s50, 32768\n\t"
    "s_nop 0\n\t"
    "buffer_load_dwordx4 %[vu0], %[rs], s98 offen sc1 lds\n\t"
    "s_add_u32 m0, s50, 33792\n\t"
    "s_nop 0\n\t"
    "buffer_load_dwordx4 %[vu1], %[rs], s98 offen sc1 lds\n\t"
    "s_add_u32 s98, s98, 458752\n\t"
    "s_waitcnt vmcnt(2)\n\t"
    "s_barrier\n\t"
    "ds_read_b128 v[84:87], %[a0]\n\t"
    "ds_read_b128 v[88:91], %[a1]\n\t"
    "s_nop 15\n\t"
    "s_nop 11\n\t"
    "ds_read_b128 v[40:43], %[lf] offset:0\n\t"
    "ds_read_b128 v[44:47], %[lf] offset:1024\n\t"
    "ds_read_b128 v[48:51], %[lf] offset:2048\n\t"
    "ds_read_b128 v[52:55], %[lf] offset:3072\n\t"
    "ds_read_b128 v[56:59], %[lf] offset:4096\n\t"
    "ds_read_b128 v[60:63], %[lf] offset:5120\n\t"
    "ds_read_b128 v[64:67], %[lf] offset:6144\n\t"
    "s_nop 7\n\t"
    "s_waitcnt lgkmcnt(0)\n\t"
    "v_smfmac_f32_16x16x64_bf16 v[68:71], v[40:43], v[128:135], %[idx]\n\t"
    "v_smfmac_f32_16x16x64_bf16 v[72:75], v[40:43], v[184:191], %[idx]\n\t"
    "v_smfmac_f32_16x16x64_bf16 v[68:71], v[44:47], v[136:143], %[idx]\n\t"
    "v_smfmac_f32_16x16x64_bf16 v[72:75], v[44:47], v[192:199], %[idx]\n\t"
    "v_smfmac_f32_16x16x64_bf16 v[68:71], v[48:51], v[144:151], %[idx]\n\t"
    "v_smfmac_f32_16x16x64_bf16 v[72:75], v[48:51], v[200:207], %[idx]\n\t"
    "v_smfmac_f32_16x16x64_bf16 v[68:71], v[52:55], v[152:159], %[idx]\n\t"
    "v_smfmac_f32_16x16x64_bf16 v[72:75], v[52:55], v[208:215], %[idx]\n\t"
    "v_smfmac_f32_16x16x64_bf16 v[68:71], v[56:59], v[160:167], %[idx]\n\t"
    "v_smfmac_f32_16x16x64_bf16 v[72:75], v[56:59], v[216:223], %[idx]\n\t"
    "s_setprio 3\n\t"
    "s_barrier\n\t"
    "ds_read_b64_tr_b16 v[92:93], %[a2] offset:7168\n\t"
    "ds_read_b64_tr_b16 v[94:95], %[a2] offset:7296\n\t"
    "ds_read_b64_tr_b16 v[96:97], %[a2] offset:8192\n\t"
    "ds_read_b64_tr_b16 v[98:99], %[a2] offset:8320\n\t"
    "v_smfmac_f32_16x16x64_bf16 v[68:71], v[60:63], v[168:175], %[idx]\n\t"
    "v_smfmac_f32_16x16x64_bf16 v[72:75], v[60:63], v[224:231], %[idx]\n\t"
    "s_waitcnt lgkmcnt(2)\n\t"
    "v_mfma_f32_16x16x32_bf16 v[100:103], v[92:95], v[84:87], 0\n\t"
    "s_waitcnt lgkmcnt(0)\n\t"
    "v_mfma_f32_16x16x32_bf16 v[100:103], v[96:99], v[88:91], v[100:103]\n\t"
    "s_nop 7\n\t"
    "v_add_f32_dpp v100, v102, v100 row_ror:8 row_mask:0xf bank_mask:0xf\n\t"
    "v_add_f32_dpp v101, v103, v101 row_ror:8 row_mask:0xf bank_mask:0xf\n\t"
    "v_cvt_pk_bf16_f32 v82, v100, v101\n\t"
    "v_min_u32 v82, -2, v82\n\t"
    "buffer_store_dword v82, %[vpub], %[rs], s37 offen sc1\n\t"
    "v_smfmac_f32_16x16x64_bf16 v[68:71], v[64:67], v[176:183], %[idx]\n\t"
    "v_smfmac_f32_16x16x64_bf16 v[72:75], v[64:67], v[232:239], %[idx]\n\t"
    "s_setprio 3\n\t"
    "s_barrier\n\t"
    "buffer_load_dwordx4 v[104:107], %[vl], %[rs], s39 offen sc1\n\t"
    "s_nop 3\n\t"
    "v_add_f32 v68, v68, v70\n\t"
    "v_add_f32 v69, v69, v71\n\t"
    "v_cvt_pk_bf16_f32 v80, v68, v69\n\t"
    "v_add_f32 v72, v72, v74\n\t"
    "v_add_f32 v73, v73, v75\n\t"
    "v_cvt_pk_bf16_f32 v81, v72, v73\n\t"
    "v_cndmask_b32_e64 v76, v80, v252, s[64:65]\n\t"
    "v_cndmask_b32_e64 v78, v81, v252, s[64:65]\n\t"
    "v_mov_b32_dpp v82, v80 quad_perm:[2,3,2,3] row_mask:0xf bank_mask:0xf\n\t"
    "v_mov_b32_dpp v83, v81 quad_perm:[2,3,2,3] row_mask:0xf bank_mask:0xf\n\t"
    "v_cndmask_b32_e64 v77, v82, v252, s[64:65]\n\t"
    "v_cndmask_b32_e64 v79, v83, v252, s[64:65]\n\t"
    "ds_write_b64 %[ll], v[76:77] offset:10240\n\t"
    "s_cmp_eq_u32 s36, 63\n\t"
    "s_cbranch_scc1 .Lend_%=\n\t"
    "s_nop 15\n\t"
    "buffer_load_dwordx4 v[108:111], %[vl], %[rs], s38 offen sc1\n\t"
    "s_waitcnt lgkmcnt(0)\n\t"
    "s_barrier\n\t"
    "s_add_u32 s36, s36, 1\n\t"
    "v_readlane_b32 s37, v123, s36\n\t"
    "buffer_store_dwordx4 v[92:95], %[a3], %[rs], s96 offen sc1\n\t"
    "buffer_store_dwordx4 v[96:99], %[a4], %[rs], s96 offen sc1\n\t"
    "s_add_u32 s96, s96, 114688\n\t"
    "buffer_store_dword v80, %[vz0], %[rs], s97 offen sc1\n\t"
    "buffer_store_dword v81, %[vz1], %[rs], s97 offen sc1\n\t"
    "s_add_u32 s97, s97, 114688\n\t"
    "s_waitcnt vmcnt(5) lgkmcnt(0)\n\t"
    ".Lme_%=:\n\t"
    "v_lshrrev_b32 v58, 4, v105\n\t"
    "v_bfi_b32 v58, s42, v104, v58\n\t"
    "v_perm_b32 v56, v58, v58, s44\n\t"
    "v_perm_b32 v57, v58, v58, s44\n\t"
    "v_perm_b32 v67, v58, v58, s44\n\t"
    "v_max3_u32 v60, v104, v105, v106\n\t"
    "v_permlane16_swap_b32 v56, v57\n\t"
    "v_max_f32 v58, v56, v57\n\t"
    "v_max_f32 v59, v56, v57\n\t"
    "v_cmp_u_f32 vcc, v60, v107\n\t"
    "s_nop 0\n\t"
    "v_permlane32_swap_b32 v58, v59\n\t"
    "s_cbranch_vccnz .Lfe_%=\n\t"
    ".Leg_%=:\n\t"
    "v_max_f32 v61, v58, v59\n\t"
    "v_sub_f32 v56, v67, v61\n\t"
    "v_add_f32 v56, 0x42c80000, v56\n\t"
    "v_cvt_u32_f32 v56, v56\n\t"
    "v_and_b32 v104, s43, v104\n\t"
    "v_mul_u32_u24 v56, 0x800080, v56\n\t"
    "v_and_b32 v105, s43, v105\n\t"
    "v_add_u32 v106, v106, v56\n\t"
    "v_add_u32 v107, v107, v56\n\t"
    "v_add_u32 v104, v104, v56\n\t"
    "v_add_u32 v105, v105, v56\n\t"
    "s_nop 1\n\t"
    "v_mfma_f32_16x16x32_bf16 v[40:43], v[248:251], v[104:107], 0\n\t"
    "v_mfma_f32_16x16x32_bf16 v[44:47], v[252:255], v[104:107], 0\n\t"
    "s_waitcnt vmcnt(4)\n\t"
    ".Lms_%=:\n\t"
    "v_mfma_f32_16x16x32_bf16 v[48:51], %[sel], v[108:111], 0\n\t"
    "v_max3_u32 v60, v108, v109, v110\n\t"
    "v_cmp_u_f32 vcc, v60, v111\n\t"
    "s_nop 1\n\t"
    "s_cbranch_vccnz .Lfs_%=\n\t"
    ".Lsg_%=:\n\t"
    "s_nop 0\n\t"
    "v_cndmask_b32_e64 v40, v40, v42, s[46:47]\n\t"
    "v_cndmask_b32_e64 v41, v41, v43, s[46:47]\n\t"
    "v_max3_f32 v56, v48, v49, v50\n\t"
    "v_max3_f32 v57, v56, v51, v61\n\t"
    "v_max3_f32 v56, v56, v51, v61\n\t"
    "s_nop 1\n\t"
    "v_permlane32_swap_b32 v56, v57\n\t"
    "v_max_f32 v62, v56, v57\n\t"
    "v_sub_f32 v48, v48, v62\n\t"
    "v_sub_f32 v49, v49, v62\n\t"
    "v_exp_f32 v48, v48\n\t"
    "v_exp_f32 v49, v49\n\t"
    "v_sub_f32 v66, v61, v62\n\t"
    "v_cvt_pk_bf16_f32 v63, v48, v49\n\t"
    "v_and_b32 v64, v63, v120\n\t"
    "v_and_b32 v65, v63, v121\n\t"
    "s_nop 1\n\t"
    "v_mfma_f32_16x16x16_bf16 v[52:55], v[78:79], v[64:65], 0\n\t"
    "v_exp_f32 v66, v66\n\t"
    "s_nop 6\n\t"
    "v_fma_f32 v56, v44, v66, v54\n\t"
    "v_rcp_f32 v56, v56\n\t"
    "v_fma_f32 v52, v40, v66, v52\n\t"
    "v_fma_f32 v53, v41, v66, v53\n\t"
    "v_mul_f32 v52, v52, v56\n\t"
    "v_mul_f32 v53, v53, v56\n\t"
    "s_nop 0\n\t"
    "v_add_f32_dpp v52, v52, v52 row_ror:8 row_mask:0xf bank_mask:0xf\n\t"
    "v_add_f32_dpp v53, v53, v53 row_ror:8 row_mask:0xf bank_mask:0xf\n\t"
    "v_cvt_pk_bf16_f32 v56, v52, v53\n\t"
    "ds_write_b32 %[lred], v56\n\t"
    "s_waitcnt lgkmcnt(0)\n\t"
    "s_barrier\n\t"
    "s_setprio 0\n\t"
    "s_cbranch_execnz .Lloop_%=\n\t"
    "s_branch .Lp2_%=\n\t"
    "s_nop 7\n\t"
    ".Lfe_%=:\n\t"
    "v_max3_u32 v60, v104, v105, v106\n\t"
    "v_max_u32 v60, v60, v107\n\t"
    "v_cmp_eq_u32 vcc, -1, v60\n\t"
    "s_nop 0\n\t"
    "s_cbranch_vccz .Leg_%=\n\t"
    "ds_write_b32 v112, v113 offset:14336\n\t"
    "s_sub_u32 %[cap], %[cap], 1\n\t"
    "s_cbranch_scc1 .Lde_%=\n\t"
    "buffer_load_dwordx4 v[104:107], %[vl], %[rs], s39 offen sc1\n\t"
    "buffer_load_dwordx4 v[108:111], %[vl], %[rs], s38 offen sc1\n\t"
    "s_waitcnt vmcnt(0)\n\t"
    "s_branch .Lme_%=\n\t"
    ".Lde_%=:\n\t"
    "s_mov_b32 %[cap], 0\n\t"
    "s_branch .Leg_%=\n\t"
    ".Lfs_%=:\n\t"
    "v_max3_u32 v60, v108, v109, v110\n\t"
    "v_max_u32 v60, v60, v111\n\t"
    "v_cmp_eq_u32 vcc, -1, v60\n\t"
    "s_nop 0\n\t"
    "s_cbranch_vccz .Lsg_%=\n\t"
    "ds_write_b32 v112, v113 offset:14336\n\t"
    "s_sub_u32 %[cap], %[cap], 1\n\t"
    "s_cbranch_scc1 .Lds_%=\n\t"
    "buffer_load_dwordx4 v[108:111], %[vl], %[rs], s38 offen sc1\n\t"
    "s_waitcnt vmcnt(0)\n\t"
    "s_branch .Lms_%=\n\t"
    ".Lds_%=:\n\t"
    "s_mov_b32 %[cap], 0\n\t"
    "s_branch .Lsg_%=\n\t"
    ".Lend_%=:\n\t"
    "buffer_store_dwordx4 v[92:95], %[a3], %[rs], s96 offen sc1\n\t"
    "buffer_store_dwordx4 v[96:99], %[a4], %[rs], s96 offen sc1\n\t"
    "s_add_u32 s96, s96, 114688\n\t"
    "buffer_store_dword v80, %[vz0], %[rs], s97 offen sc1\n\t"
    "buffer_store_dword v81, %[vz1], %[rs], s97 offen sc1\n\t"
    "s_add_u32 s97, s97, 114688\n\t"
    "s_waitcnt vmcnt(0) lgkmcnt(0)\n\t"
    "s_barrier\n\t"
    "s_mov_b32 m0, s40\n\t"
    "s_setprio 0\n\t"
    HELP_OPS);
  if (K == 1) asm volatile(
    "s_mov_b32 s40, m0\n\t"
    "buffer_load_dword v83, %[vt], %[rs], 0 offen sc1\n\t"
    "s_mov_b32 s46, 0xff00ff00\n\t"
    "s_mov_b32 s47, 0xff00ff00\n\t"
    "s_mov_b32 s41, 0\n\t"
    "s_mov_b32 s42, 0xf000f000\n\t"
    "s_mov_b32 s43, 0x0fff0fff\n\t"
    "s_mov_b32 s44, 0x03010c0c\n\t"
    "v_mov_b32 v252, 0x3f803f80\n\t"
    "v_mov_b32 v253, 0x3f803f80\n\t"
    "v_mov_b32 v254, 0x3f803f80\n\t"
    "v_mov_b32 v255, 0x3f803f80\n\t"
    "s_mov_b32 s50, 512\n\t"
    "v_mov_b32 v113, s50\n\t"
    "v_mov_b32 v112, %[lb]\n\t"
    "v_cndmask_b32_e64 v120, -1, 0, s[46:47]\n\t"
    "v_cndmask_b32_e64 v121, 0, -1, s[46:47]\n\t"
    "s_mov_b32 s64, 0xcccccccc\n\t"
    "s_mov_b32 s65, 0xcccccccc\n\t"
    "v_mov_b32 v44, -1\n\t"
    "v_mov_b32 v45, -1\n\t"
    "v_mov_b32 v46, -1\n\t"
    "v_mov_b32 v47, -1\n\t"
    "s_mov_b32 s51, -1\n\t"
    "s_mov_b32 s36, 1\n\t"
    "s_mov_b32 s37, 0\n\t"
    "s_waitcnt vmcnt(0)\n\t"
    "s_nop 15\n\t"
    "s_nop 13\n\t"
    ".Lpr_%=:\n\t"
    "s_lshl_b32 s50, s36, 13\n\t"
    "s_add_u32 s50, s50, s37\n\t"
    "s_add_u32 s50, s50, %[pb]\n\t"
    "s_memtime s[96:97]\n\t"
    "s_waitcnt lgkmcnt(0)\n\t"
    "buffer_store_dwordx4 v[44:47], %[vp], %[rs], s50 offen sc1\n\t"
    "s_waitcnt vmcnt(0)\n\t"
    "s_memtime s[98:99]\n\t"
    "s_waitcnt lgkmcnt(0)\n\t"
    "s_sub_u32 s50, s98, s96\n\t"
    "s_lshl_b32 s50, s50, 2\n\t"
    "s_or_b32 s50, s50, s36\n\t"
    "s_min_u32 s51, s51, s50\n\t"
    "s_add_u32 s36, s36, 1\n\t"
    "s_and_b32 s36, s36, 3\n\t"
    "s_add_u32 s37, s37, 2176\n\t"
    "s_cmp_lt_u32 s37, 8704\n\t"
    "s_cbranch_scc1 .Lpr_%=\n\t"
    "v_mov_b32 v40, s51\n\t"
    "ds_write_b32 v112, v40 offset:10244\n\t"
    "s_waitcnt lgkmcnt(0)\n\t"
    "s_barrier\n\t"
    "ds_read_b128 v[40:43], v112 offset:10240\n\t"
    "s_waitcnt lgkmcnt(0)\n\t"
    "v_min3_u32 v40, v40, v41, v42\n\t"
    "v_min_u32 v40, v40, v43\n\t"
    "s_nop 0\n\t"
    "v_readfirstlane_b32 s51, v40\n\t"
    "s_and_b32 s51, s51, 3\n\t"
    "s_lshl_b32 s51, s51, 13\n\t"
    "s_add_u32 s51, s51, %[pb]\n\t"
    "s_add_u32 s50, s51, 0\n\t"
    "buffer_store_dwordx4 v[44:47], %[vi0], %[rs], s50 offen sc1\n\t"
    "s_add_u32 s50, s51, 4096\n\t"
    "buffer_store_dwordx4 v[44:47], %[vi0], %[rs], s50 offen sc1\n\t"
    "s_add_u32 s50, s51, 32768\n\t"
    "buffer_store_dwordx4 v[44:47], %[vi], %[rs], s50 offen sc1\n\t"
    "s_add_u32 s50, s51, 36864\n\t"
    "buffer_store_dwordx4 v[44:47], %[vi], %[rs], s50 offen sc1\n\t"
    "s_add_u32 s50, s51, 65536\n\t"
    "buffer_store_dwordx4 v[44:47], %[vi], %[rs], s50 offen sc1\n\t"
    "s_add_u32 s50, s51, 69632\n\t"
    "buffer_store_dwordx4 v[44:47], %[vi], %[rs], s50 offen sc1\n\t"
    "s_getpc_b64 s[48:49]\n\t"
    ".Lwp_%=:\n\t"
    "v_mbcnt_lo_u32_b32 v82, -1, 0\n\t"
    "v_mbcnt_hi_u32_b32 v82, -1, v82\n\t"
    "v_lshlrev_b32 v82, 7, v82\n\t"
    "v_min_u32 v82, .Lend_%=-.Lwp_%=, v82\n\t"
    "global_load_dword v82, v82, s[48:49]\n\t"
    "s_or_b32 s51, s51, 1\n\t"
    "v_mov_b32 v123, s51\n\t"
    "s_waitcnt vmcnt(1)\n\t"
    "s_barrier\n\t"
    "v_mov_b32 v114, 0\n\t"
    "v_mov_b32 v122, 0\n\t"
    "s_mov_b32 s96, 0\n\t"
    "s_mov_b32 s97, 0\n\t"
    "s_mov_b32 s98, 0\n\t"
    "s_mov_b32 s99, 1\n\t"
    "s_mov_b32 s41, 0\n\t"
    "s_mov_b32 s36, 0\n\t"
    "s_mov_b32 s37, 0\n\t"
    "s_mov_b64 exec, 0\n\t"
    "s_branch .Lloop_%=\n\t"
    ".Lp2_%=:\n\t"
    "s_mov_b64 exec, -1\n\t"
    "s_mov_b32 s96, 0\n\t"
    "s_mov_b32 s97, 0\n\t"
    "s_mov_b32 s98, 0\n\t"
    "s_mov_b32 s99, 1\n\t"
    "s_mov_b32 s41, 0\n\t"
    "s_add_u32 s50, %[oR], 0\n\t"
    "buffer_load_dwordx4 v[128:131], %[vwa], %[rs], s50 offen offset:0\n\t"
    "buffer_load_dwordx4 v[132:135], %[vwa], %[rs], s50 offen offset:1024\n\t"
    "s_add_u32 s50, %[oR], 2048\n\t"
    "buffer_load_dwordx4 v[136:139], %[vwa], %[rs], s50 offen offset:0\n\t"
    "buffer_load_dwordx4 v[140:143], %[vwa], %[rs], s50 offen offset:1024\n\t"
    "s_add_u32 s50, %[oR], 4096\n\t"
    "buffer_load_dwordx4 v[144:147], %[vwa], %[rs], s50 offen offset:0\n\t"
    "buffer_load_dwordx4 v[148:151], %[vwa], %[rs], s50 offen offset:1024\n\t"
    "s_add_u32 s50, %[oL], 0\n\t"
    "buffer_load_dwordx4 v[152:155], %[vwa], %[rs], s50 offen offset:0\n\t"
    "buffer_load_dwordx4 v[156:159], %[vwa], %[rs], s50 offen offset:1024\n\t"
    "s_add_u32 s50, %[oL], 2048\n\t"
    "buffer_load_dwordx4 v[160:163], %[vwa], %[rs], s50 offen offset:0\n\t"
    "buffer_load_dwordx4 v[164:167], %[vwa], %[rs], s50 offen offset:1024\n\t"
    "s_add_u32 s50, %[oL], 4096\n\t"
    "buffer_load_dwordx4 v[168:171], %[vwa], %[rs], s50 offen offset:0\n\t"
    "buffer_load_dwordx4 v[172:175], %[vwa], %[rs], s50 offen offset:1024\n\t"
    "s_add_u32 s50, %[oM], 0\n\t"
    "buffer_load_dwordx4 v[176:179], %[vwa], %[rs], s50 offen offset:0\n\t"
    "buffer_load_dwordx4 v[180:183], %[vwa], %[rs], s50 offen offset:1024\n\t"
    "s_add_u32 s50, %[oR], 0\n\t"
    "buffer_load_dwordx4 v[184:187], %[vwb], %[rs], s50 offen offset:0\n\t"
    "buffer_load_dwordx4 v[188:191], %[vwb], %[rs], s50 offen offset:1024\n\t"
    "s_add_u32 s50, %[oR], 2048\n\t"
    "buffer_load_dwordx4 v[192:195], %[vwb], %[rs], s50 offen offset:0\n\t"
    "buffer_load_dwordx4 v[196:199], %[vwb], %[rs], s50 offen offset:1024\n\t"
    "s_add_u32 s50, %[oR], 4096\n\t"
    "buffer_load_dwordx4 v[200:203], %[vwb], %[rs], s50 offen offset:0\n\t"
    "buffer_load_dwordx4 v[204:207], %[vwb], %[rs], s50 offen offset:1024\n\t"
    "s_add_u32 s50, %[oL], 0\n\t"
    "buffer_load_dwordx4 v[208:211], %[vwb], %[rs], s50 offen offset:0\n\t"
    "buffer_load_dwordx4 v[212:215], %[vwb], %[rs], s50 offen offset:1024\n\t"
    "s_add_u32 s50, %[oL], 2048\n\t"
    "buffer_load_dwordx4 v[216:219], %[vwb], %[rs], s50 offen offset:0\n\t"
    "buffer_load_dwordx4 v[220:223], %[vwb], %[rs], s50 offen offset:1024\n\t"
    "s_add_u32 s50, %[oL], 4096\n\t"
    "buffer_load_dwordx4 v[224:227], %[vwb], %[rs], s50 offen offset:0\n\t"
    "buffer_load_dwordx4 v[228:231], %[vwb], %[rs], s50 offen offset:1024\n\t"
    "s_add_u32 s50, %[oM], 0\n\t"
    "buffer_load_dwordx4 v[232:235], %[vwb], %[rs], s50 offen offset:0\n\t"
    "buffer_load_dwordx4 v[236:239], %[vwb], %[rs], s50 offen offset:1024\n\t"
    ".Ltb_%=:\n\t"
    "buffer_load_dword v123, %[vtab], %[rs], 0 offen sc1\n\t"
    "s_waitcnt vmcnt(0)\n\t"
    "v_cmp_eq_u32 vcc, 0, v123\n\t"
    "s_cbranch_vccz .Ltq_%=\n\t"
    "s_sub_u32 %[cap], %[cap], 1\n\t"
    "s_cbranch_scc0 .Ltb_%=\n\t"
    "s_mov_b32 %[cap], 0\n\t"
    ".Ltq_%=:\n\t"
    "v_and_b32 v123, -2, v123\n\t"
    "s_mov_b32 s36, 0\n\t"
    "s_nop 0\n\t"
    "v_readlane_b32 s37, v123, 0\n\t"
    "s_mov_b32 s36, 0\n\t"
    "v_readlane_b32 s37, v123, 0\n\t"
    "s_barrier\n\t"
    "s_add_u32 s50, s41, %[lb]\n\t"
    "s_add_u32 m0, s50, 34816\n\t"
    "s_nop 0\n\t"
    "buffer_load_dwordx4 %[vu0], %[rs], s98 offen sc1 lds\n\t"
    "s_add_u32 m0, s50, 35840\n\t"
    "s_nop 0\n\t"
    "buffer_load_dwordx4 %[vu1], %[rs], s98 offen sc1 lds\n\t"
    "s_add_u32 s98, s98, 458752\n\t"
    "s_waitcnt vmcnt(0)\n\t"
    "s_barrier\n\t"
    ".Lloop_%=:\n\t"
    "s_add_u32 s38, s37, 0x8000\n\t"
    "s_add_u32 s39, s37, 0x10000\n\t"
    "v_add_u32 v124, s41, %[ll]\n\t"
    "s_add_u32 s41, s41, 0x4000\n\t"
    "s_and_b32 s41, s41, 0xffff\n\t"
    "ds_read_b128 v[248:251], v124 offset:35840\n\t"
    "v_mov_b64 v[68:69], 0\n\t"
    "v_mov_b64 v[70:71], 0\n\t"
    "v_mov_b64 v[72:73], 0\n\t"
    "v_mov_b64 v[74:75], 0\n\t"
    "s_barrier\n\t"
    "s_nop 15\n\t"
    "s_nop 7\n\t"
    "s_add_u32 s50, s41, %[lb]\n\t"
    "s_add_u32 m0, s50, 34816\n\t"
    "s_nop 0\n\t"
    "buffer_load_dwordx4 %[vu0], %[rs], s98 offen sc1 lds\n\t"
    "s_add_u32 m0, s50, 35840\n\t"
    "s_nop 0\n\t"
    "buffer_load_dwordx4 %[vu1], %[rs], s98 offen sc1 lds\n\t"
    "s_add_u32 s98, s98, 458752\n\t"
    "s_waitcnt vmcnt(2)\n\t"
    "s_barrier\n\t"
    "s_nop 15\n\t"
    "s_nop 11\n\t"
    "ds_read_b128 v[40:43], %[lf] offset:0\n\t"
    "ds_read_b128 v[44:47], %[lf] offset:1024\n\t"
    "ds_read_b128 v[48:51], %[lf] offset:2048\n\t"
    "ds_read_b128 v[52:55], %[lf] offset:3072\n\t"
    "ds_read_b128 v[56:59], %[lf] offset:4096\n\t"
    "ds_read_b128 v[60:63], %[lf] offset:5120\n\t"
    "ds_read_b128 v[64:67], %[lf] offset:6144\n\t"
    "s_nop 7\n\t"
    "s_waitcnt lgkmcnt(0)\n\t"
    "v_smfmac_f32_16x16x64_bf16 v[68:71], v[40:43], v[128:135], %[idx]\n\t"
    "v_smfmac_f32_16x16x64_bf16 v[72:75], v[40:43], v[184:191], %[idx]\n\t"
    "v_smfmac_f32_16x16x64_bf16 v[68:71], v[44:47], v[136:143], %[idx]\n\t"
    "v_smfmac_f32_16x16x64_bf16 v[72:75], v[44:47], v[192:199], %[idx]\n\t"
    "v_smfmac_f32_16x16x64_bf16 v[68:71], v[48:51], v[144:151], %[idx]\n\t"
    "v_smfmac_f32_16x16x64_bf16 v[72:75], v[48:51], v[200:207], %[idx]\n\t"
    "v_smfmac_f32_16x16x64_bf16 v[68:71], v[52:55], v[152:159], %[idx]\n\t"
    "v_smfmac_f32_16x16x64_bf16 v[72:75], v[52:55], v[208:215], %[idx]\n\t"
    "v_smfmac_f32_16x16x64_bf16 v[68:71], v[56:59], v[160:167], %[idx]\n\t"
    "v_smfmac_f32_16x16x64_bf16 v[72:75], v[56:59], v[216:223], %[idx]\n\t"
    "s_barrier\n\t"
    "v_smfmac_f32_16x16x64_bf16 v[68:71], v[60:63], v[168:175], %[idx]\n\t"
    "v_smfmac_f32_16x16x64_bf16 v[72:75], v[60:63], v[224:231], %[idx]\n\t"
    "v_smfmac_f32_16x16x64_bf16 v[68:71], v[64:67], v[176:183], %[idx]\n\t"
    "v_smfmac_f32_16x16x64_bf16 v[72:75], v[64:67], v[232:239], %[idx]\n\t"
    "s_setprio 3\n\t"
    "s_barrier\n\t"
    "buffer_load_dwordx4 v[104:107], %[vl], %[rs], s39 offen sc1\n\t"
    "s_nop 3\n\t"
    "v_add_f32 v68, v68, v70\n\t"
    "v_add_f32 v69, v69, v71\n\t"
    "v_cvt_pk_bf16_f32 v80, v68, v69\n\t"
    "v_add_f32 v72, v72, v74\n\t"
    "v_add_f32 v73, v73, v75\n\t"
    "v_cvt_pk_bf16_f32 v81, v72, v73\n\t"
    "v_cndmask_b32_e64 v76, v80, v252, s[64:65]\n\t"
    "v_cndmask_b32_e64 v78, v81, v252, s[64:65]\n\t"
    "v_mov_b32_dpp v82, v80 quad_perm:[2,3,2,3] row_mask:0xf bank_mask:0xf\n\t"
    "v_mov_b32_dpp v83, v81 quad_perm:[2,3,2,3] row_mask:0xf bank_mask:0xf\n\t"
    "v_cndmask_b32_e64 v77, v82, v252, s[64:65]\n\t"
    "v_cndmask_b32_e64 v79, v83, v252, s[64:65]\n\t"
    "ds_write_b64 %[ll], v[76:77] offset:11264\n\t"
    "s_cmp_eq_u32 s36, 63\n\t"
    "s_cbranch_scc1 .Lend_%=\n\t"
    "s_nop 15\n\t"
    "buffer_load_dwordx4 v[108:111], %[vl], %[rs], s38 offen sc1\n\t"
    "s_waitcnt lgkmcnt(0)\n\t"
    "s_barrier\n\t"
    "s_add_u32 s36, s36, 1\n\t"
    "v_readlane_b32 s37, v123, s36\n\t"
    "v_mov_b64 v[92:93], 0\n\t"
    "v_mov_b64 v[94:95], 0\n\t"
    "buffer_store_dword v80, %[vz0], %[rs], s97 offen sc1\n\t"
    "buffer_store_dword v81, %[vz1], %[rs], s97 offen sc1\n\t"
    "s_add_u32 s97, s97, 114688\n\t"
    "s_waitcnt vmcnt(3) lgkmcnt(0)\n\t"
    ".Lme_%=:\n\t"
    "v_lshrrev_b32 v58, 4, v105\n\t"
    "v_bfi_b32 v58, s42, v104, v58\n\t"
    "v_perm_b32 v56, v58, v58, s44\n\t"
    "v_perm_b32 v57, v58, v58, s44\n\t"
    "v_perm_b32 v67, v58, v58, s44\n\t"
    "v_max3_u32 v60, v104, v105, v106\n\t"
    "v_permlane16_swap_b32 v56, v57\n\t"
    "v_max_f32 v58, v56, v57\n\t"
    "v_max_f32 v59, v56, v57\n\t"
    "v_cmp_u_f32 vcc, v60, v107\n\t"
    "s_nop 0\n\t"
    "v_permlane32_swap_b32 v58, v59\n\t"
    "s_cbranch_vccnz .Lfe_%=\n\t"
    ".Leg_%=:\n\t"
    "v_max_f32 v61, v58, v59\n\t"
    "v_sub_f32 v56, v67, v61\n\t"
    "v_add_f32 v56, 0x42c80000, v56\n\t"
    "v_cvt_u32_f32 v56, v56\n\t"
    "v_and_b32 v104, s43, v104\n\t"
    "v_mul_u32_u24 v56, 0x800080, v56\n\t"
    "v_and_b32 v105, s43, v105\n\t"
    "v_add_u32 v106, v106, v56\n\t"
    "v_add_u32 v107, v107, v56\n\t"
    "v_add_u32 v104, v104, v56\n\t"
    "v_add_u32 v105, v105, v56\n\t"
    "s_nop 1\n\t"
    "v_mfma_f32_16x16x32_bf16 v[40:43], v[248:251], v[104:107], 0\n\t"
    "v_mfma_f32_16x16x32_bf16 v[44:47], v[252:255], v[104:107], 0\n\t"
    "s_waitcnt vmcnt(2)\n\t"
    ".Lms_%=:\n\t"
    "v_mfma_f32_16x16x32_bf16 v[48:51], %[sel], v[108:111], 0\n\t"
    "v_max3_u32 v60, v108, v109, v110\n\t"
    "v_cmp_u_f32 vcc, v60, v111\n\t"
    "s_nop 1\n\t"
    "s_cbranch_vccnz .Lfs_%=\n\t"
    ".Lsg_%=:\n\t"
    "s_nop 0\n\t"
    "v_cndmask_b32_e64 v40, v40, v42, s[46:47]\n\t"
    "v_cndmask_b32_e64 v41, v41, v43, s[46:47]\n\t"
    "v_max3_f32 v56, v48, v49, v50\n\t"
    "v_max3_f32 v57, v56, v51, v61\n\t"
    "v_max3_f32 v56, v56, v51, v61\n\t"
    "s_nop 1\n\t"
    "v_permlane32_swap_b32 v56, v57\n\t"
    "v_max_f32 v62, v56, v57\n\t"
    "v_sub_f32 v48, v48, v62\n\t"
    "v_sub_f32 v49, v49, v62\n\t"
    "v_exp_f32 v48, v48\n\t"
    "v_exp_f32 v49, v49\n\t"
    "v_sub_f32 v66, v61, v62\n\t"
    "v_cvt_pk_bf16_f32 v63, v48, v49\n\t"
    "v_and_b32 v64, v63, v120\n\t"
    "v_and_b32 v65, v63, v121\n\t"
    "s_nop 1\n\t"
    "v_mfma_f32_16x16x16_bf16 v[52:55], v[78:79], v[64:65], 0\n\t"
    "v_exp_f32 v66, v66\n\t"
    "s_nop 6\n\t"
    "v_fma_f32 v56, v44, v66, v54\n\t"
    "v_rcp_f32 v56, v56\n\t"
    "v_fma_f32 v52, v40, v66, v52\n\t"
    "v_fma_f32 v53, v41, v66, v53\n\t"
    "v_mul_f32 v52, v52, v56\n\t"
    "v_mul_f32 v53, v53, v56\n\t"
    "s_nop 0\n\t"
    "v_add_f32_dpp v52, v52, v52 row_ror:8 row_mask:0xf bank_mask:0xf\n\t"
    "v_add_f32_dpp v53, v53, v53 row_ror:8 row_mask:0xf bank_mask:0xf\n\t"
    "v_cvt_pk_bf16_f32 v56, v52, v53\n\t"
    "ds_write_b32 %[lred], v56\n\t"
    "s_waitcnt lgkmcnt(0)\n\t"
    "s_barrier\n\t"
    "ds_read_b128 v[84:87], %[a0]\n\t"
    "ds_read_b128 v[88:91], %[a0] offset:512\n\t"
    "s_waitcnt lgkmcnt(0)\n\t"
    "v_smfmac_f32_16x16x64_bf16 v[92:95], %[a5], v[84:91], %[a1]\n\t"
    "s_nop 7\n\t"
    "v_cvt_pk_bf16_f32 v82, v92, v93\n\t"
    "v_min_u32 v82, -2, v82\n\t"
    "buffer_store_dword v82, %[vpub], %[rs], s37 offen sc1\n\t"
    "s_setprio 0\n\t"
    "s_cbranch_execnz .Lloop_%=\n\t"
    "s_branch .Lp2_%=\n\t"
    "s_nop 7\n\t"
    ".Lfe_%=:\n\t"
    "v_max3_u32 v60, v104, v105, v106\n\t"
    "v_max_u32 v60, v60, v107\n\t"
    "v_cmp_eq_u32 vcc, -1, v60\n\t"
    "s_nop 0\n\t"
    "s_cbranch_vccz .Leg_%=\n\t"
    "ds_write_b32 v112, v113 offset:14336\n\t"
    "s_sub_u32 %[cap], %[cap], 1\n\t"
    "s_cbranch_scc1 .Lde_%=\n\t"
    "buffer_load_dwordx4 v[104:107], %[vl], %[rs], s39 offen sc1\n\t"
    "buffer_load_dwordx4 v[108:111], %[vl], %[rs], s38 offen sc1\n\t"
    "s_waitcnt vmcnt(0)\n\t"
    "s_branch .Lme_%=\n\t"
    ".Lde_%=:\n\t"
    "s_mov_b32 %[cap], 0\n\t"
    "s_branch .Leg_%=\n\t"
    ".Lfs_%=:\n\t"
    "v_max3_u32 v60, v108, v109, v110\n\t"
    "v_max_u32 v60, v60, v111\n\t"
    "v_cmp_eq_u32 vcc, -1, v60\n\t"
    "s_nop 0\n\t"
    "s_cbranch_vccz .Lsg_%=\n\t"
    "ds_write_b32 v112, v113 offset:14336\n\t"
    "s_sub_u32 %[cap], %[cap], 1\n\t"
    "s_cbranch_scc1 .Lds_%=\n\t"
    "buffer_load_dwordx4 v[108:111], %[vl], %[rs], s38 offen sc1\n\t"
    "s_waitcnt vmcnt(0)\n\t"
    "s_branch .Lms_%=\n\t"
    ".Lds_%=:\n\t"
    "s_mov_b32 %[cap], 0\n\t"
    "s_branch .Lsg_%=\n\t"
    ".Lend_%=:\n\t"
    "buffer_store_dword v80, %[vz0], %[rs], s97 offen sc1\n\t"
    "buffer_store_dword v81, %[vz1], %[rs], s97 offen sc1\n\t"
    "s_add_u32 s97, s97, 114688\n\t"
    "s_waitcnt vmcnt(0) lgkmcnt(0)\n\t"
    "s_barrier\n\t"
    "s_mov_b32 m0, s40\n\t"
    "s_setprio 0\n\t"
    HELP_OPS);
  if (K == 2) asm volatile(
    "s_mov_b32 s40, m0\n\t"
    "buffer_load_dword v83, %[vt], %[rs], 0 offen sc1\n\t"
    "s_mov_b32 s46, 0xff00ff00\n\t"
    "s_mov_b32 s47, 0xff00ff00\n\t"
    "s_mov_b32 s41, 0\n\t"
    "s_mov_b32 s42, 0xf000f000\n\t"
    "s_mov_b32 s43, 0x0fff0fff\n\t"
    "s_mov_b32 s44, 0x03010c0c\n\t"
    "v_mov_b32 v252, 0x3f803f80\n\t"
    "v_mov_b32 v253, 0x3f803f80\n\t"
    "v_mov_b32 v254, 0x3f803f80\n\t"
    "v_mov_b32 v255, 0x3f803f80\n\t"
    "s_mov_b32 s50, 512\n\t"
    "v_mov_b32 v113, s50\n\t"
    "v_mov_b32 v112, %[lb]\n\t"
    "v_cndmask_b32_e64 v120, -1, 0, s[46:47]\n\t"
    "v_cndmask_b32_e64 v121, 0, -1, s[46:47]\n\t"
    "s_mov_b32 s64, 0xcccccccc\n\t"
    "s_mov_b32 s65, 0xcccccccc\n\t"
    "v_mov_b32 v44, -1\n\t"
    "v_mov_b32 v45, -1\n\t"
    "v_mov_b32 v46, -1\n\t"
    "v_mov_b32 v47, -1\n\t"
    "s_mov_b32 s51, -1\n\t"
    "s_mov_b32 s36, 2\n\t"
    "s_mov_b32 s37, 0\n\t"
    "s_waitcnt vmcnt(0)\n\t"
    "s_nop 15\n\t"
    "s_nop 15\n\t"
    "s_nop 15\n\t"
    "s_nop 11\n\t"
    ".Lpr_%=:\n\t"
    "s_lshl_b32 s50, s36, 13\n\t"
    "s_add_u32 s50, s50, s37\n\t"
    "s_add_u32 s50, s50, %[pb]\n\t"
    "s_memtime s[96:97]\n\t"
    "s_waitcnt lgkmcnt(0)\n\t"
    "buffer_store_dwordx4 v[44:47], %[vp], %[rs], s50 offen sc1\n\t"
    "s_waitcnt vmcnt(0)\n\t"
    "s_memtime s[98:99]\n\t"
    "s_waitcnt lgkmcnt(0)\n\t"
    "s_sub_u32 s50, s98, s96\n\t"
    "s_lshl_b32 s50, s50, 2\n\t"
    "s_or_b32 s50, s50, s36\n\t"
    "s_min_u32 s51, s51, s50\n\t"
    "s_add_u32 s36, s36, 1\n\t"
    "s_and_b32 s36, s36, 3\n\t"
    "s_add_u32 s37, s37, 2176\n\t"
    "s_cmp_lt_u32 s37, 8704\n\t"
    "s_cbranch_scc1 .Lpr_%=\n\t"
    "v_mov_b32 v40, s51\n\t"
    "ds_write_b32 v112, v40 offset:10248\n\t"
    "s_waitcnt lgkmcnt(0)\n\t"
    "s_barrier\n\t"
    "ds_read_b128 v[40:43], v112 offset:10240\n\t"
    "s_waitcnt lgkmcnt(0)\n\t"
    "v_min3_u32 v40, v40, v41, v42\n\t"
    "v_min_u32 v40, v40, v43\n\t"
    "s_nop 0\n\t"
    "v_readfirstlane_b32 s51, v40\n\t"
    "s_and_b32 s51, s51, 3\n\t"
    "s_lshl_b32 s51, s51, 13\n\t"
    "s_add_u32 s51, s51, %[pb]\n\t"
    "s_add_u32 s50, s51, 0\n\t"
    "buffer_store_dwordx4 v[44:47], %[vi0], %[rs], s50 offen sc1\n\t"
    "s_add_u32 s50, s51, 4096\n\t"
    "buffer_store_dwordx4 v[44:47], %[vi0], %[rs], s50 offen sc1\n\t"
    "s_add_u32 s50, s51, 32768\n\t"
    "buffer_store_dwordx4 v[44:47], %[vi], %[rs], s50 offen sc1\n\t"
    "s_add_u32 s50, s51, 36864\n\t"
    "buffer_store_dwordx4 v[44:47], %[vi], %[rs], s50 offen sc1\n\t"
    "s_add_u32 s50, s51, 65536\n\t"
    "buffer_store_dwordx4 v[44:47], %[vi], %[rs], s50 offen sc1\n\t"
    "s_add_u32 s50, s51, 69632\n\t"
    "buffer_store_dwordx4 v[44:47], %[vi], %[rs], s50 offen sc1\n\t"
    "s_getpc_b64 s[48:49]\n\t"
    ".Lwp_%=:\n\t"
    "v_mbcnt_lo_u32_b32 v82, -1, 0\n\t"
    "v_mbcnt_hi_u32_b32 v82, -1, v82\n\t"
    "v_lshlrev_b32 v82, 7, v82\n\t"
    "v_min_u32 v82, .Lend_%=-.Lwp_%=, v82\n\t"
    "global_load_dword v82, v82, s[48:49]\n\t"
    "s_or_b32 s51, s51, 1\n\t"
    "v_mov_b32 v123, s51\n\t"
    "s_waitcnt vmcnt(1)\n\t"
    "s_barrier\n\t"
    "v_mov_b32 v114, 0\n\t"
    "v_mov_b32 v122, 0\n\t"
    "s_mov_b32 s96, 0\n\t"
    "s_mov_b32 s97, 0\n\t"
    "s_mov_b32 s98, 0\n\t"
    "s_mov_b32 s99, 1\n\t"
    "s_mov_b32 s41, 0\n\t"
    "s_mov_b32 s36, 0\n\t"
    "s_mov_b32 s37, 0\n\t"
    "s_mov_b64 exec, 0\n\t"
    "s_branch .Lloop_%=\n\t"
    ".Lp2_%=:\n\t"
    "s_mov_b64 exec, -1\n\t"
    "s_mov_b32 s96, 0\n\t"
    "s_mov_b32 s97, 0\n\t"
    "s_mov_b32 s98, 0\n\t"
    "s_mov_b32 s99, 1\n\t"
    "s_mov_b32 s41, 0\n\t"
    "s_add_u32 s50, %[oR], 0\n\t"
    "buffer_load_dwordx4 v[128:131], %[vwa], %[rs], s50 offen offset:0\n\t"
    "buffer_load_dwordx4 v[132:135], %[vwa], %[rs], s50 offen offset:1024\n\t"
    "s_add_u32 s50, %[oR], 2048\n\t"
    "buffer_load_dwordx4 v[136:139], %[vwa], %[rs], s50 offen offset:0\n\t"
    "buffer_load_dwordx4 v[140:143], %[vwa], %[rs], s50 offen offset:1024\n\t"
    "s_add_u32 s50, %[oR], 4096\n\t"
    "buffer_load_dwordx4 v[144:147], %[vwa], %[rs], s50 offen offset:0\n\t"
    "buffer_load_dwordx4 v[148:151], %[vwa], %[rs], s50 offen offset:1024\n\t"
    "s_add_u32 s50, %[oL], 0\n\t"
    "buffer_load_dwordx4 v[152:155], %[vwa], %[rs], s50 offen offset:0\n\t"
    "buffer_load_dwordx4 v[156:159], %[vwa], %[rs], s50 offen offset:1024\n\t"
    "s_add_u32 s50, %[oL], 2048\n\t"
    "buffer_load_dwordx4 v[160:163], %[vwa], %[rs], s50 offen offset:0\n\t"
    "buffer_load_dwordx4 v[164:167], %[vwa], %[rs], s50 offen offset:1024\n\t"
    "s_add_u32 s50, %[oL], 4096\n\t"
    "buffer_load_dwordx4 v[168:171], %[vwa], %[rs], s50 offen offset:0\n\t"
    "buffer_load_dwordx4 v[172:175], %[vwa], %[rs], s50 offen offset:1024\n\t"
    "s_add_u32 s50, %[oM], 0\n\t"
    "buffer_load_dwordx4 v[176:179], %[vwa], %[rs], s50 offen offset:0\n\t"
    "buffer_load_dwordx4 v[180:183], %[vwa], %[rs], s50 offen offset:1024\n\t"
    "s_add_u32 s50, %[oR], 0\n\t"
    "buffer_load_dwordx4 v[184:187], %[vwb], %[rs], s50 offen offset:0\n\t"
    "buffer_load_dwordx4 v[188:191], %[vwb], %[rs], s50 offen offset:1024\n\t"
    "s_add_u32 s50, %[oR], 2048\n\t"
    "buffer_load_dwordx4 v[192:195], %[vwb], %[rs], s50 offen offset:0\n\t"
    "buffer_load_dwordx4 v[196:199], %[vwb], %[rs], s50 offen offset:1024\n\t"
    "s_add_u32 s50, %[oR], 4096\n\t"
    "buffer_load_dwordx4 v[200:203], %[vwb], %[rs], s50 offen offset:0\n\t"
    "buffer_load_dwordx4 v[204:207], %[vwb], %[rs], s50 offen offset:1024\n\t"
    "s_add_u32 s50, %[oL], 0\n\t"
    "buffer_load_dwordx4 v[208:211], %[vwb], %[rs], s50 offen offset:0\n\t"
    "buffer_load_dwordx4 v[212:215], %[vwb], %[rs], s50 offen offset:1024\n\t"
    "s_add_u32 s50, %[oL], 2048\n\t"
    "buffer_load_dwordx4 v[216:219], %[vwb], %[rs], s50 offen offset:0\n\t"
    "buffer_load_dwordx4 v[220:223], %[vwb], %[rs], s50 offen offset:1024\n\t"
    "s_add_u32 s50, %[oL], 4096\n\t"
    "buffer_load_dwordx4 v[224:227], %[vwb], %[rs], s50 offen offset:0\n\t"
    "buffer_load_dwordx4 v[228:231], %[vwb], %[rs], s50 offen offset:1024\n\t"
    "s_add_u32 s50, %[oM], 0\n\t"
    "buffer_load_dwordx4 v[232:235], %[vwb], %[rs], s50 offen offset:0\n\t"
    "buffer_load_dwordx4 v[236:239], %[vwb], %[rs], s50 offen offset:1024\n\t"
    "buffer_load_dword v122, %[a1], %[rs], 0 offen sc1\n\t"
    ".Ltb_%=:\n\t"
    "buffer_load_dword v123, %[vtab], %[rs], 0 offen sc1\n\t"
    "s_waitcnt vmcnt(0)\n\t"
    "v_cmp_eq_u32 vcc, 0, v123\n\t"
    "s_cbranch_vccz .Ltq_%=\n\t"
    "s_sub_u32 %[cap], %[cap], 1\n\t"
    "s_cbranch_scc0 .Ltb_%=\n\t"
    "s_mov_b32 %[cap], 0\n\t"
    ".Ltq_%=:\n\t"
    "v_and_b32 v123, -2, v123\n\t"
    "s_mov_b32 s36, 0\n\t"
    "s_nop 0\n\t"
    "v_readlane_b32 s37, v123, 0\n\t"
    "s_mov_b32 s36, 0\n\t"
    "v_readlane_b32 s37, v123, 0\n\t"
    "s_waitcnt vmcnt(0)\n\t"
    "v_cmp_gt_u32 vcc, s99, v122\n\t"
    "s_nop 0\n\t"
    "s_cbranch_vccz .LfkP_%=\n\t"
    ".LflP_%=:\n\t"
    "buffer_load_dword v122, %[a1], %[rs], 0 offen sc1\n\t"
    "s_waitcnt vmcnt(0)\n\t"
    "v_cmp_gt_u32 vcc, s99, v122\n\t"
    "s_cbranch_vccz .LfkP_%=\n\t"
    "s_sleep 1\n\t"
    "s_sub_u32 %[cap], %[cap], 1\n\t"
    "s_cbranch_scc0 .LflP_%=\n\t"
    "s_mov_b32 %[cap], 0\n\t"
    ".LfkP_%=:\n\t"
    "s_barrier\n\t"
    "s_add_u32 s50, s41, %[lb]\n\t"
    "s_add_u32 m0, s50, 36864\n\t"
    "s_nop 0\n\t"
    "buffer_load_dwordx4 %[vu0], %[rs], s98 offen sc1 lds\n\t"
    "s_add_u32 m0, s50, 37888\n\t"
    "s_nop 0\n\t"
    "buffer_load_dwordx4 %[vu1], %[rs], s98 offen sc1 lds\n\t"
    "s_add_u32 s98, s98, 458752\n\t"
    "s_waitcnt vmcnt(0)\n\t"
    "s_barrier\n\t"
    ".Lloop_%=:\n\t"
    "s_nop 15\n\t"
    "s_nop 11\n\t"
    "s_add_u32 s38, s37, 0x8000\n\t"
    "s_add_u32 s39, s37, 0x10000\n\t"
    "v_add_u32 v124, s41, %[ll]\n\t"
    "s_add_u32 s41, s41, 0x4000\n\t"
    "s_and_b32 s41, s41, 0xffff\n\t"
    "ds_read_b128 v[248:251], v124 offset:37888\n\t"
    "v_mov_b64 v[68:69], 0\n\t"
    "v_mov_b64 v[70:71], 0\n\t"
    "v_mov_b64 v[72:73], 0\n\t"
    "v_mov_b64 v[74:75], 0\n\t"
    "s_barrier\n\t"
    "s_nop 15\n\t"
    "s_nop 7\n\t"
    "buffer_load_dword v122, %[a1], %[rs], 0 offen sc1\n\t"
    "s_add_u32 s50, s41, %[lb]\n\t"
    "s_add_u32 m0, s50, 36864\n\t"
    "s_nop 0\n\t"
    "buffer_load_dwordx4 %[vu0], %[rs], s98 offen sc1 lds\n\t"
    "s_add_u32 m0, s50, 37888\n\t"
    "s_nop 0\n\t"
    "buffer_load_dwordx4 %[vu1], %[rs], s98 offen sc1 lds\n\t"
    "s_add_u32 s98, s98, 458752\n\t"
    "s_waitcnt vmcnt(3)\n\t"
    "s_barrier\n\t"
    "s_cmp_eq_u32 s36, 63\n\t"
    "s_cbranch_scc1 .Ll1_%=\n\t"
    ".Ll2_%=:\n\t"
    "s_nop 15\n\t"
    "s_nop 11\n\t"
    "ds_read_b128 v[40:43], %[lf] offset:0\n\t"
    "ds_read_b128 v[44:47], %[lf] offset:1024\n\t"
    "ds_read_b128 v[48:51], %[lf] offset:2048\n\t"
    "ds_read_b128 v[52:55], %[lf] offset:3072\n\t"
    "ds_read_b128 v[56:59], %[lf] offset:4096\n\t"
    "ds_read_b128 v[60:63], %[lf] offset:5120\n\t"
    "ds_read_b128 v[64:67], %[lf] offset:6144\n\t"
    "s_nop 7\n\t"
    "s_waitcnt lgkmcnt(0)\n\t"
    "v_smfmac_f32_16x16x64_bf16 v[68:71], v[40:43], v[128:135], %[idx]\n\t"
    "v_smfmac_f32_16x16x64_bf16 v[72:75], v[40:43], v[184:191], %[idx]\n\t"
    "v_smfmac_f32_16x16x64_bf16 v[68:71], v[44:47], v[136:143], %[idx]\n\t"
    "v_smfmac_f32_16x16x64_bf16 v[72:75], v[44:47], v[192:199], %[idx]\n\t"
    "v_smfmac_f32_16x16x64_bf16 v[68:71], v[48:51], v[144:151], %[idx]\n\t"
    "v_smfmac_f32_16x16x64_bf16 v[72:75], v[48:51], v[200:207], %[idx]\n\t"
    "v_smfmac_f32_16x16x64_bf16 v[68:71], v[52:55], v[152:159], %[idx]\n\t"
    "v_smfmac_f32_16x16x64_bf16 v[72:75], v[52:55], v[208:215], %[idx]\n\t"
    "v_smfmac_f32_16x16x64_bf16 v[68:71], v[56:59], v[160:167], %[idx]\n\t"
    "v_smfmac_f32_16x16x64_bf16 v[72:75], v[56:59], v[216:223], %[idx]\n\t"
    "s_barrier\n\t"
    "v_smfmac_f32_16x16x64_bf16 v[68:71], v[60:63], v[168:175], %[idx]\n\t"
    "v_smfmac_f32_16x16x64_bf16 v[72:75], v[60:63], v[224:231], %[idx]\n\t"
    "v_smfmac_f32_16x16x64_bf16 v[68:71], v[64:67], v[176:183], %[idx]\n\t"
    "v_smfmac_f32_16x16x64_bf16 v[72:75], v[64:67], v[232:239], %[idx]\n\t"
    "s_cmp_eq_u32 s36, 63\n\t"
    "s_cbranch_scc1 .LfkL_%=\n\t"
    "s_add_u32 s99, s36, 4\n\t"
    "s_lshr_b32 s99, s99, 1\n\t"
    "s_min_u32 s99, s99, 32\n\t"
    "s_waitcnt vmcnt(2)\n\t"
    "v_cmp_gt_u32 vcc, s99, v122\n\t"
    "s_nop 0\n\t"
    "s_cbranch_vccnz .LflL_%=\n\t"
    ".LfkL_%=:\n\t"
    "s_setprio 3\n\t"
    "s_barrier\n\t"
    "buffer_load_dwordx4 v[104:107], %[vl], %[rs], s39 offen sc1\n\t"
    "s_nop 3\n\t"
    "v_add_f32 v68, v68, v70\n\t"
    "v_add_f32 v69, v69, v71\n\t"
    "v_cvt_pk_bf16_f32 v80, v68, v69\n\t"
    "v_add_f32 v72, v72, v74\n\t"
    "v_add_f32 v73, v73, v75\n\t"
    "v_cvt_pk_bf16_f32 v81, v72, v73\n\t"
    "v_cndmask_b32_e64 v76, v80, v252, s[64:65]\n\t"
    "v_cndmask_b32_e64 v78, v81, v252, s[64:65]\n\t"
    "v_mov_b32_dpp v82, v80 quad_perm:[2,3,2,3] row_mask:0xf bank_mask:0xf\n\t"
    "v_mov_b32_dpp v83, v81 quad_perm:[2,3,2,3] row_mask:0xf bank_mask:0xf\n\t"
    "v_cndmask_b32_e64 v77, v82, v252, s[64:65]\n\t"
    "v_cndmask_b32_e64 v79, v83, v252, s[64:65]\n\t"
    "ds_write_b64 %[ll], v[76:77] offset:12288\n\t"
    "s_cmp_eq_u32 s36, 63\n\t"
    "s_cbranch_scc1 .Lend_%=\n\t"
    "v_mov_b32 v82, s36\n\t"
    "s_nop 15\n\t"
    "buffer_load_dwordx4 v[108:111], %[vl], %[rs], s38 offen sc1\n\t"
    "s_waitcnt lgkmcnt(0)\n\t"
    "s_barrier\n\t"
    "s_add_u32 s36, s36, 1\n\t"
    "v_readlane_b32 s37, v123, s36\n\t"
    "buffer_store_dword v80, %[vz0], %[rs], s97 offen sc1\n\t"
    "buffer_store_dword v81, %[vz1], %[rs], s97 offen sc1\n\t"
    "s_add_u32 s97, s97, 114688\n\t"
    "buffer_store_dword v82, %[a0], %[rs], 0 offen sc1\n\t"
    "s_waitcnt vmcnt(4) lgkmcnt(0)\n\t"
    ".Lme_%=:\n\t"
    "v_lshrrev_b32 v58, 4, v105\n\t"
    "v_bfi_b32 v58, s42, v104, v58\n\t"
    "v_perm_b32 v56, v58, v58, s44\n\t"
    "v_perm_b32 v57, v58, v58, s44\n\t"
    "v_perm_b32 v67, v58, v58, s44\n\t"
    "v_max3_u32 v60, v104, v105, v106\n\t"
    "v_permlane16_swap_b32 v56, v57\n\t"
    "v_max_f32 v58, v56, v57\n\t"
    "v_max_f32 v59, v56, v57\n\t"
    "v_cmp_u_f32 vcc, v60, v107\n\t"
    "s_nop 0\n\t"
    "v_permlane32_swap_b32 v58, v59\n\t"
    "s_cbranch_vccnz .Lfe_%=\n\t"
    ".Leg_%=:\n\t"
    "v_max_f32 v61, v58, v59\n\t"
    "v_sub_f32 v56, v67, v61\n\t"
    "v_add_f32 v56, 0x42c80000, v56\n\t"
    "v_cvt_u32_f32 v56, v56\n\t"
    "v_and_b32 v104, s43, v104\n\t"
    "v_mul_u32_u24 v56, 0x800080, v56\n\t"
    "v_and_b32 v105, s43, v105\n\t"
    "v_add_u32 v106, v106, v56\n\t"
    "v_add_u32 v107, v107, v56\n\t"
    "v_add_u32 v104, v104, v56\n\t"
    "v_add_u32 v105, v105, v56\n\t"
    "s_nop 1\n\t"
    "v_mfma_f32_16x16x32_bf16 v[40:43], v[248:251], v[104:107], 0\n\t"
    "v_mfma_f32_16x16x32_bf16 v[44:47], v[252:255], v[104:107], 0\n\t"
    "s_waitcnt vmcnt(3)\n\t"
    ".Lms_%=:\n\t"
    "v_mfma_f32_16x16x32_bf16 v[48:51], %[sel], v[108:111], 0\n\t"
    "v_max3_u32 v60, v108, v109, v110\n\t"
    "v_cmp_u_f32 vcc, v60, v111\n\t"
    "s_nop 1\n\t"
    "s_cbranch_vccnz .Lfs_%=\n\t"
    ".Lsg_%=:\n\t"
    "s_nop 0\n\t"
    "v_cndmask_b32_e64 v40, v40, v42, s[46:47]\n\t"
    "v_cndmask_b32_e64 v41, v41, v43, s[46:47]\n\t"
    "v_max3_f32 v56, v48, v49, v50\n\t"
    "v_max3_f32 v57, v56, v51, v61\n\t"
    "v_max3_f32 v56, v56, v51, v61\n\t"
    "s_nop 1\n\t"
    "v_permlane32_swap_b32 v56, v57\n\t"
    "v_max_f32 v62, v56, v57\n\t"
    "v_sub_f32 v48, v48, v62\n\t"
    "v_sub_f32 v49, v49, v62\n\t"
    "v_exp_f32 v48, v48\n\t"
    "v_exp_f32 v49, v49\n\t"
    "v_sub_f32 v66, v61, v62\n\t"
    "v_cvt_pk_bf16_f32 v63, v48, v49\n\t"
    "v_and_b32 v64, v63, v120\n\t"
    "v_and_b32 v65, v63, v121\n\t"
    "s_nop 1\n\t"
    "v_mfma_f32_16x16x16_bf16 v[52:55], v[78:79], v[64:65], 0\n\t"
    "v_exp_f32 v66, v66\n\t"
    "s_nop 6\n\t"
    "v_fma_f32 v56, v44, v66, v54\n\t"
    "v_rcp_f32 v56, v56\n\t"
    "v_fma_f32 v52, v40, v66, v52\n\t"
    "v_fma_f32 v53, v41, v66, v53\n\t"
    "v_mul_f32 v52, v52, v56\n\t"
    "v_mul_f32 v53, v53, v56\n\t"
    "s_nop 0\n\t"
    "v_add_f32_dpp v52, v52, v52 row_ror:8 row_mask:0xf bank_mask:0xf\n\t"
    "v_add_f32_dpp v53, v53, v53 row_ror:8 row_mask:0xf bank_mask:0xf\n\t"
    "v_cvt_pk_bf16_f32 v56, v52, v53\n\t"
    "ds_write_b32 %[lred], v56\n\t"
    "s_waitcnt lgkmcnt(0)\n\t"
    "s_barrier\n\t"
    "s_setprio 0\n\t"
    "s_cbranch_execnz .Lloop_%=\n\t"
    "s_branch .Lp2_%=\n\t"
    "s_nop 7\n\t"
    ".LflL_%=:\n\t"
    "buffer_load_dword v122, %[a1], %[rs], 0 offen sc1\n\t"
    "s_waitcnt vmcnt(0)\n\t"
    "v_cmp_gt_u32 vcc, s99, v122\n\t"
    "s_cbranch_vccz .LfkL_%=\n\t"
    "s_sleep 1\n\t"
    "s_sub_u32 %[cap], %[cap], 1\n\t"
    "s_cbranch_scc0 .LflL_%=\n\t"
    "s_mov_b32 %[cap], 0\n\t"
    "s_branch .LfkL_%=\n\t"
    ".Ll1_%=:\n\t"
    "v_mov_b32 v82, 63\n\t"
    "buffer_store_dword v82, %[a0], %[rs], 0 offen sc1\n\t"
    "s_branch .Ll2_%=\n\t"
    ".Lfe_%=:\n\t"
    "v_max3_u32 v60, v104, v105, v106\n\t"
    "v_max_u32 v60, v60, v107\n\t"
    "v_cmp_eq_u32 vcc, -1, v60\n\t"
    "s_nop 0\n\t"
    "s_cbranch_vccz .Leg_%=\n\t"
    "ds_write_b32 v112, v113 offset:14336\n\t"
    "s_sub_u32 %[cap], %[cap], 1\n\t"
    "s_cbranch_scc1 .Lde_%=\n\t"
    "buffer_load_dwordx4 v[104:107], %[vl], %[rs], s39 offen sc1\n\t"
    "buffer_load_dwordx4 v[108:111], %[vl], %[rs], s38 offen sc1\n\t"
    "s_waitcnt vmcnt(0)\n\t"
    "s_branch .Lme_%=\n\t"
    ".Lde_%=:\n\t"
    "s_mov_b32 %[cap], 0\n\t"
    "s_branch .Leg_%=\n\t"
    ".Lfs_%=:\n\t"
    "v_max3_u32 v60, v108, v109, v110\n\t"
    "v_max_u32 v60, v60, v111\n\t"
    "v_cmp_eq_u32 vcc, -1, v60\n\t"
    "s_nop 0\n\t"
    "s_cbranch_vccz .Lsg_%=\n\t"
    "ds_write_b32 v112, v113 offset:14336\n\t"
    "s_sub_u32 %[cap], %[cap], 1\n\t"
    "s_cbranch_scc1 .Lds_%=\n\t"
    "buffer_load_dwordx4 v[108:111], %[vl], %[rs], s38 offen sc1\n\t"
    "s_waitcnt vmcnt(0)\n\t"
    "s_branch .Lms_%=\n\t"
    ".Lds_%=:\n\t"
    "s_mov_b32 %[cap], 0\n\t"
    "s_branch .Lsg_%=\n\t"
    ".Lend_%=:\n\t"
    "buffer_store_dword v80, %[vz0], %[rs], s97 offen sc1\n\t"
    "buffer_store_dword v81, %[vz1], %[rs], s97 offen sc1\n\t"
    "s_add_u32 s97, s97, 114688\n\t"
    "s_waitcnt vmcnt(0) lgkmcnt(0)\n\t"
    "s_barrier\n\t"
    "s_mov_b32 m0, s40\n\t"
    "s_setprio 0\n\t"
    "v_mov_b32 v82, 64\n\t"
    "buffer_store_dword v82, %[a0], %[rs], 0 offen sc1\n\t"
    HELP_OPS);
  if (K == 3) asm volatile(
    "s_mov_b32 s40, m0\n\t"
    "buffer_load_dword v83, %[vt], %[rs], 0 offen sc1\n\t"
    "s_getpc_b64 s[48:49]\n\t"
    ".Lpc_%=:\n\t"
    "s_mov_b32 s50, 0\n\t"
    "s_sub_u32 s50, .Lsl7_%=-.Lpc_%=, s50\n\t"
    "s_add_u32 s52, s48, s50\n\t"
    "s_addc_u32 s53, s49, 0\n\t"
    "s_mov_b32 s46, 0xff00ff00\n\t"
    "s_mov_b32 s47, 0xff00ff00\n\t"
    "s_mov_b32 s41, 0\n\t"
    "s_mov_b32 s42, 0xf000f000\n\t"
    "s_mov_b32 s43, 0x0fff0fff\n\t"
    "s_mov_b32 s44, 0x03010c0c\n\t"
    "v_mov_b32 v252, 0x3f803f80\n\t"
    "v_mov_b32 v253, 0x3f803f80\n\t"
    "v_mov_b32 v254, 0x3f803f80\n\t"
    "v_mov_b32 v255, 0x3f803f80\n\t"
    "s_mov_b32 s50, 512\n\t"
    "v_mov_b32 v113, s50\n\t"
    "v_mov_b32 v112, %[lb]\n\t"
    "v_cndmask_b32_e64 v120, -1, 0, s[46:47]\n\t"
    "v_cndmask_b32_e64 v121, 0, -1, s[46:47]\n\t"
    "s_mov_b32 s64, 0xcccccccc\n\t"
    "s_mov_b32 s65, 0xcccccccc\n\t"
    "v_mov_b32 v44, -1\n\t"
    "v_mov_b32 v45, -1\n\t"
    "v_mov_b32 v46, -1\n\t"
    "v_mov_b32 v47, -1\n\t"
    "s_mov_b32 s51, -1\n\t"
    "s_mov_b32 s36, 3\n\t"
    "s_mov_b32 s37, 0\n\t"
    "s_waitcnt vmcnt(0)\n\t"
    "s_nop 15\n\t"
    "s_nop 15\n\t"
    "s_nop 15\n\t"
    "s_nop 15\n\t"
    "s_nop 15\n\t"
    "s_nop 9\n\t"
    ".Lpr_%=:\n\t"
    "s_lshl_b32 s50, s36, 13\n\t"
    "s_add_u32 s50, s50, s37\n\t"
    "s_add_u32 s50, s50, %[pb]\n\t"
    "s_memtime s[96:97]\n\t"
    "s_waitcnt lgkmcnt(0)\n\t"
    "buffer_store_dwordx4 v[44:47], %[vp], %[rs], s50 offen sc1\n\t"
    "s_waitcnt vmcnt(0)\n\t"
    "s_memtime s[98:99]\n\t"
    "s_waitcnt lgkmcnt(0)\n\t"
    "s_sub_u32 s50, s98, s96\n\t"
    "s_lshl_b32 s50, s50, 2\n\t"
    "s_or_b32 s50, s50, s36\n\t"
    "s_min_u32 s51, s51, s50\n\t"
    "s_add_u32 s36, s36, 1\n\t"
    "s_and_b32 s36, s36, 3\n\t"
    "s_add_u32 s37, s37, 2176\n\t"
    "s_cmp_lt_u32 s37, 8704\n\t"
    "s_cbranch_scc1 .Lpr_%=\n\t"
    "v_mov_b32 v40, s51\n\t"
    "ds_write_b32 v112, v40 offset:10252\n\t"
    "s_waitcnt lgkmcnt(0)\n\t"
    "s_barrier\n\t"
    "ds_read_b128 v[40:43], v112 offset:10240\n\t"
    "s_waitcnt lgkmcnt(0)\n\t"
    "v_min3_u32 v40, v40, v41, v42\n\t"
    "v_min_u32 v40, v40, v43\n\t"
    "s_nop 0\n\t"
    "v_readfirstlane_b32 s51, v40\n\t"
    "s_and_b32 s51, s51, 3\n\t"
    "s_lshl_b32 s51, s51, 13\n\t"
    "s_add_u32 s51, s51, %[pb]\n\t"
    "s_add_u32 s50, s51, 0\n\t"
    "buffer_store_dwordx4 v[44:47], %[vi0], %[rs], s50 offen sc1\n\t"
    "s_add_u32 s50, s51, 4096\n\t"
    "buffer_store_dwordx4 v[44:47], %[vi0], %[rs], s50 offen sc1\n\t"
    "s_add_u32 s50, s51, 32768\n\t"
    "buffer_store_dwordx4 v[44:47], %[vi], %[rs], s50 offen sc1\n\t"
    "s_add_u32 s50, s51, 36864\n\t"
    "buffer_store_dwordx4 v[44:47], %[vi], %[rs], s50 offen sc1\n\t"
    "s_add_u32 s50, s51, 65536\n\t"
    "buffer_store_dwordx4 v[44:47], %[vi], %[rs], s50 offen sc1\n\t"
    "s_add_u32 s50, s51, 69632\n\t"
    "buffer_store_dwordx4 v[44:47], %[vi], %[rs], s50 offen sc1\n\t"
    "s_getpc_b64 s[48:49]\n\t"
    ".Lwp_%=:\n\t"
    "v_mbcnt_lo_u32_b32 v82, -1, 0\n\t"
    "v_mbcnt_hi_u32_b32 v82, -1, v82\n\t"
    "v_lshlrev_b32 v82, 7, v82\n\t"
    "v_min_u32 v82, .Lend_%=-.Lwp_%=, v82\n\t"
    "global_load_dword v82, v82, s[48:49]\n\t"
    "s_or_b32 s51, s51, 1\n\t"
    "v_mov_b32 v123, s51\n\t"
    "s_waitcnt vmcnt(1)\n\t"
    "s_barrier\n\t"
    "v_mov_b32 v114, 0\n\t"
    "v_mov_b32 v122, 0\n\t"
    "s_mov_b32 s96, 0\n\t"
    "s_mov_b32 s97, 0\n\t"
    "s_mov_b32 s98, 0\n\t"
    "s_mov_b32 s99, 1\n\t"
    "s_mov_b32 s41, 0\n\t"
    "s_mov_b32 s99, 1136\n\t"
    "s_memrealtime s[62:63]\n\t"
    "s_waitcnt lgkmcnt(0)\n\t"
    "s_sub_u32 s96, s62, 64\n\t"
    "s_mov_b32 s36, 0\n\t"
    "s_mov_b32 s37, 0\n\t"
    "s_mov_b64 exec, 0\n\t"
    "s_branch .Lloop_%=\n\t"
    ".Lp2_%=:\n\t"
    "s_mov_b64 exec, -1\n\t"
    "s_mov_b32 s96, 0\n\t"
    "s_mov_b32 s97, 0\n\t"
    "s_mov_b32 s98, 0\n\t"
    "s_mov_b32 s99, 1\n\t"
    "s_mov_b32 s41, 0\n\t"
    "s_mov_b32 s99, 1136\n\t"
    "s_memrealtime s[62:63]\n\t"
    "s_waitcnt lgkmcnt(0)\n\t"
    "s_sub_u32 s96, s62, 64\n\t"
    "s_add_u32 s50, %[oR], 0\n\t"
    "buffer_load_dwordx4 v[128:131], %[vwa], %[rs], s50 offen offset:0\n\t"
    "buffer_load_dwordx4 v[132:135], %[vwa], %[rs], s50 offen offset:1024\n\t"
    "s_add_u32 s50, %[oR], 2048\n\t"
    "buffer_load_dwordx4 v[136:139], %[vwa], %[rs], s50 offen offset:0\n\t"
    "buffer_load_dwordx4 v[140:143], %[vwa], %[rs], s50 offen offset:1024\n\t"
    "s_add_u32 s50, %[oR], 4096\n\t"
    "buffer_load_dwordx4 v[144:147], %[vwa], %[rs], s50 offen offset:0\n\t"
    "buffer_load_dwordx4 v[148:151], %[vwa], %[rs], s50 offen offset:1024\n\t"
    "s_add_u32 s50, %[oL], 0\n\t"
    "buffer_load_dwordx4 v[152:155], %[vwa], %[rs], s50 offen offset:0\n\t"
    "buffer_load_dwordx4 v[156:159], %[vwa], %[rs], s50 offen offset:1024\n\t"
    "s_add_u32 s50, %[oL], 2048\n\t"
    "buffer_load_dwordx4 v[160:163], %[vwa], %[rs], s50 offen offset:0\n\t"
    "buffer_load_dwordx4 v[164:167], %[vwa], %[rs], s50 offen offset:1024\n\t"
    "s_add_u32 s50, %[oL], 4096\n\t"
    "buffer_load_dwordx4 v[168:171], %[vwa], %[rs], s50 offen offset:0\n\t"
    "buffer_load_dwordx4 v[172:175], %[vwa], %[rs], s50 offen offset:1024\n\t"
    "s_add_u32 s50, %[oM], 0\n\t"
    "buffer_load_dwordx4 v[176:179], %[vwa], %[rs], s50 offen offset:0\n\t"
    "buffer_load_dwordx4 v[180:183], %[vwa], %[rs], s50 offen offset:1024\n\t"
    "s_add_u32 s50, %[oR], 0\n\t"
    "buffer_load_dwordx4 v[184:187], %[vwb], %[rs], s50 offen offset:0\n\t"
    "buffer_load_dwordx4 v[188:191], %[vwb], %[rs], s50 offen offset:1024\n\t"
    "s_add_u32 s50, %[oR], 2048\n\t"
    "buffer_load_dwordx4 v[192:195], %[vwb], %[rs], s50 offen offset:0\n\t"
    "buffer_load_dwordx4 v[196:199], %[vwb], %[rs], s50 offen offset:1024\n\t"
    "s_add_u32 s50, %[oR], 4096\n\t"
    "buffer_load_dwordx4 v[200:203], %[vwb], %[rs], s50 offen offset:0\n\t"
    "buffer_load_dwordx4 v[204:207], %[vwb], %[rs], s50 offen offset:1024\n\t"
    "s_add_u32 s50, %[oL], 0\n\t"
    "buffer_load_dwordx4 v[208:211], %[vwb], %[rs], s50 offen offset:0\n\t"
    "buffer_load_dwordx4 v[212:215], %[vwb], %[rs], s50 offen offset:1024\n\t"
    "s_add_u32 s50, %[oL], 2048\n\t"
    "buffer_load_dwordx4 v[216:219], %[vwb], %[rs], s50 offen offset:0\n\t"
    "buffer_load_dwordx4 v[220:223], %[vwb], %[rs], s50 offen offset:1024\n\t"
    "s_add_u32 s50, %[oL], 4096\n\t"
    "buffer_load_dwordx4 v[224:227], %[vwb], %[rs], s50 offen offset:0\n\t"
    "buffer_load_dwordx4 v[228:231], %[vwb], %[rs], s50 offen offset:1024\n\t"
    "s_add_u32 s50, %[oM], 0\n\t"
    "buffer_load_dwordx4 v[232:235], %[vwb], %[rs], s50 offen offset:0\n\t"
    "buffer_load_dwordx4 v[236:239], %[vwb], %[rs], s50 offen offset:1024\n\t"
    ".Ltb_%=:\n\t"
    "buffer_load_dword v123, %[vtab], %[rs], 0 offen sc1\n\t"
    "s_waitcnt vmcnt(0)\n\t"
    "v_cmp_eq_u32 vcc, 0, v123\n\t"
    "s_cbranch_vccz .Ltq_%=\n\t"
    "s_sub_u32 %[cap], %[cap], 1\n\t"
    "s_cbranch_scc0 .Ltb_%=\n\t"
    "s_mov_b32 %[cap], 0\n\t"
    ".Ltq_%=:\n\t"
    "v_and_b32 v123, -2, v123\n\t"
    "s_mov_b32 s36, 0\n\t"
    "s_nop 0\n\t"
    "v_readlane_b32 s37, v123, 0\n\t"
    "s_mov_b32 s36, 0\n\t"
    "v_readlane_b32 s37, v123, 0\n\t"
    "s_barrier\n\t"
    "s_add_u32 s50, s41, %[lb]\n\t"
    "s_add_u32 m0, s50, 38912\n\t"
    "s_nop 0\n\t"
    "buffer_load_dwordx4 %[vu0], %[rs], s98 offen sc1 lds\n\t"
    "s_add_u32 m0, s50, 39936\n\t"
    "s_nop 0\n\t"
    "buffer_load_dwordx4 %[vu1], %[rs], s98 offen sc1 lds\n\t"
    "s_add_u32 s98, s98, 458752\n\t"
    "s_waitcnt vmcnt(0)\n\t"
    "s_barrier\n\t"
    ".Lloop_%=:\n\t"
    "s_nop 15\n\t"
    "s_add_u32 s38, s37, 0x8000\n\t"
    "s_add_u32 s39, s37, 0x10000\n\t"
    "v_add_u32 v124, s41, %[ll]\n\t"
    "s_add_u32 s41, s41, 0x4000\n\t"
    "s_and_b32 s41, s41, 0xffff\n\t"
    "ds_read_b128 v[248:251], v124 offset:39936\n\t"
    "v_mov_b64 v[68:69], 0\n\t"
    "v_mov_b64 v[70:71], 0\n\t"
    "v_mov_b64 v[72:73], 0\n\t"
    "v_mov_b64 v[74:75], 0\n\t"
    "s_mov_b32 s51, 255\n\t"
    "s_sub_u32 s45, s96, 3\n\t"
    ".Lsp_%=:\n\t"
    "s_memrealtime s[62:63]\n\t"
    "s_sub_u32 s50, s62, s45\n\t"
    "s_cmp_ge_i32 s50, 0\n\t"
    "s_cbranch_scc1 .Lsq_%=\n\t"
    "s_sub_u32 s51, s51, 1\n\t"
    "s_cbranch_scc0 .Lsp_%=\n\t"
    ".Lsq_%=:\n\t"
    "s_cmp_gt_i32 s50, 8\n\t"
    "s_cbranch_scc1 .Llt_%=\n\t"
    ".Llu_%=:\n\t"
    "s_min_u32 s50, s50, 3\n\t"
    "s_mul_i32 s50, s50, 12\n\t"
    "s_sub_u32 s50, 36, s50\n\t"
    "s_sub_u32 s48, s52, s50\n\t"
    "s_subb_u32 s49, s53, 0\n\t"
    "s_setpc_b64 s[48:49]\n\t"
    ".rept 9\n\t"
    "s_nop 1\n\t"
    ".endr\n\t"
    ".Lsl7_%=:\n\t"
    "s_barrier\n\t"
    "s_nop 15\n\t"
    "s_nop 7\n\t"
    "s_setprio 0\n\t"
    "s_add_u32 s50, s41, %[lb]\n\t"
    "s_add_u32 m0, s50, 38912\n\t"
    "s_nop 0\n\t"
    "buffer_load_dwordx4 %[vu0], %[rs], s98 offen sc1 lds\n\t"
    "s_add_u32 m0, s50, 39936\n\t"
    "s_nop 0\n\t"
    "buffer_load_dwordx4 %[vu1], %[rs], s98 offen sc1 lds\n\t"
    "s_add_u32 s98, s98, 458752\n\t"
    "s_waitcnt vmcnt(2)\n\t"
    "s_barrier\n\t"
    "s_waitcnt lgkmcnt(0)\n\t"
    "s_memrealtime s[62:63]\n\t"
    "s_mov_b32 s45, s37\n\t"
    "s_sub_u32 s51, s51, %[tz]\n\t"
    "s_max_i32 s51, s51, -4\n\t"
    "s_min_i32 s51, s51, 4\n\t"
    "s_mul_i32 s51, s51, 3\n\t"
    "s_nop 15\n\t"
    "s_nop 11\n\t"
    "ds_read_b32 v114, v112 offset:14336\n\t"
    "ds_read_b128 v[40:43], %[lf] offset:0\n\t"
    "ds_read_b128 v[44:47], %[lf] offset:1024\n\t"
    "ds_read_b128 v[48:51], %[lf] offset:2048\n\t"
    "ds_read_b128 v[52:55], %[lf] offset:3072\n\t"
    "ds_read_b128 v[56:59], %[lf] offset:4096\n\t"
    "ds_read_b128 v[60:63], %[lf] offset:5120\n\t"
    "ds_read_b128 v[64:67], %[lf] offset:6144\n\t"
    "s_nop 11\n\t"
    "s_waitcnt lgkmcnt(0)\n\t"
    "v_smfmac_f32_16x16x64_bf16 v[68:71], v[40:43], v[128:135], %[idx]\n\t"
    "v_readfirstlane_b32 s50, v114\n\t"
    "s_cmp_lg_u32 s50, 0\n\t"
    "s_cselect_b32 s51, 16, s51\n\t"
    "s_add_u32 s99, s99, s51\n\t"
    "s_min_u32 s99, s99, 1600\n\t"
    "v_smfmac_f32_16x16x64_bf16 v[72:75], v[40:43], v[184:191], %[idx]\n\t"
    "s_max_u32 s99, s99, 800\n\t"
    "s_lshr_b32 s51, s99, 3\n\t"
    "s_add_u32 s96, s96, s51\n\t"
    "s_mul_i32 s50, s50, 11\n\t"
    "s_lshr_b32 s50, s50, 8\n\t"
    "v_smfmac_f32_16x16x64_bf16 v[68:71], v[44:47], v[136:143], %[idx]\n\t"
    "s_add_u32 s50, s50, s62\n\t"
    "s_add_u32 s50, s50, s51\n\t"
    "s_sub_u32 s50, s50, %[ta]\n\t"
    "s_sub_u32 s51, s50, s96\n\t"
    "s_cmp_gt_i32 s51, 0\n\t"
    "v_smfmac_f32_16x16x64_bf16 v[72:75], v[44:47], v[192:199], %[idx]\n\t"
    "s_cselect_b32 s50, s50, s96\n\t"
    "s_min_u32 s50, s50, -2\n\t"
    "v_mov_b32 v82, s50\n\t"
    "buffer_store_dword v82, %[a0], %[rs], s37 offen sc1\n\t"
    "s_barrier\n\t"
    "v_smfmac_f32_16x16x64_bf16 v[68:71], v[48:51], v[144:151], %[idx]\n\t"
    "v_smfmac_f32_16x16x64_bf16 v[72:75], v[48:51], v[200:207], %[idx]\n\t"
    "v_smfmac_f32_16x16x64_bf16 v[68:71], v[52:55], v[152:159], %[idx]\n\t"
    "v_smfmac_f32_16x16x64_bf16 v[72:75], v[52:55], v[208:215], %[idx]\n\t"
    "v_smfmac_f32_16x16x64_bf16 v[68:71], v[56:59], v[160:167], %[idx]\n\t"
    "v_smfmac_f32_16x16x64_bf16 v[72:75], v[56:59], v[216:223], %[idx]\n\t"
    "v_smfmac_f32_16x16x64_bf16 v[68:71], v[60:63], v[168:175], %[idx]\n\t"
    "v_smfmac_f32_16x16x64_bf16 v[72:75], v[60:63], v[224:231], %[idx]\n\t"
    "v_smfmac_f32_16x16x64_bf16 v[68:71], v[64:67], v[176:183], %[idx]\n\t"
    "v_smfmac_f32_16x16x64_bf16 v[72:75], v[64:67], v[232:239], %[idx]\n\t"
    "s_setprio 3\n\t"
    "s_barrier\n\t"
    "buffer_load_dwordx4 v[104:107], %[vl], %[rs], s39 offen sc1\n\t"
    "s_nop 3\n\t"
    "v_add_f32 v68, v68, v70\n\t"
    "v_add_f32 v69, v69, v71\n\t"
    "v_cvt_pk_bf16_f32 v80, v68, v69\n\t"
    "v_add_f32 v72, v72, v74\n\t"
    "v_add_f32 v73, v73, v75\n\t"
    "v_cvt_pk_bf16_f32 v81, v72, v73\n\t"
    "v_cndmask_b32_e64 v76, v80, v252, s[64:65]\n\t"
    "v_cndmask_b32_e64 v78, v81, v252, s[64:65]\n\t"
    "v_mov_b32_dpp v82, v80 quad_perm:[2,3,2,3] row_mask:0xf bank_mask:0xf\n\t"
    "v_mov_b32_dpp v83, v81 quad_perm:[2,3,2,3] row_mask:0xf bank_mask:0xf\n\t"
    "v_cndmask_b32_e64 v77, v82, v252, s[64:65]\n\t"
    "v_cndmask_b32_e64 v79, v83, v252, s[64:65]\n\t"
    "ds_write_b64 %[ll], v[76:77] offset:13312\n\t"
    "s_cmp_eq_u32 s36, 63\n\t"
    "s_cbranch_scc1 .Lend_%=\n\t"
    "s_nop 15\n\t"
    "buffer_load_dwordx4 v[108:111], %[vl], %[rs], s38 offen sc1\n\t"
    "buffer_load_dword v122, %[a1], %[rs], s45 offen sc1\n\t"
    "s_waitcnt lgkmcnt(0)\n\t"
    "s_barrier\n\t"
    "s_add_u32 s36, s36, 1\n\t"
    "v_readlane_b32 s37, v123, s36\n\t"
    "buffer_store_dword v80, %[vz0], %[rs], s97 offen sc1\n\t"
    "buffer_store_dword v81, %[vz1], %[rs], s97 offen sc1\n\t"
    "s_add_u32 s97, s97, 114688\n\t"
    "s_waitcnt vmcnt(4) lgkmcnt(0)\n\t"
    ".Lme_%=:\n\t"
    "v_lshrrev_b32 v58, 4, v105\n\t"
    "v_bfi_b32 v58, s42, v104, v58\n\t"
    "v_perm_b32 v56, v58, v58, s44\n\t"
    "v_perm_b32 v57, v58, v58, s44\n\t"
    "v_perm_b32 v67, v58, v58, s44\n\t"
    "v_max3_u32 v60, v104, v105, v106\n\t"
    "v_permlane16_swap_b32 v56, v57\n\t"
    "v_max_f32 v58, v56, v57\n\t"
    "v_max_f32 v59, v56, v57\n\t"
    "v_cmp_u_f32 vcc, v60, v107\n\t"
    "s_nop 0\n\t"
    "v_permlane32_swap_b32 v58, v59\n\t"
    "s_cbranch_vccnz .Lfe_%=\n\t"
    ".Leg_%=:\n\t"
    "v_max_f32 v61, v58, v59\n\t"
    "v_sub_f32 v56, v67, v61\n\t"
    "v_add_f32 v56, 0x42c80000, v56\n\t"
    "v_cvt_u32_f32 v56, v56\n\t"
    "v_and_b32 v104, s43, v104\n\t"
    "v_mul_u32_u24 v56, 0x800080, v56\n\t"
    "v_and_b32 v105, s43, v105\n\t"
    "v_add_u32 v106, v106, v56\n\t"
    "v_add_u32 v107, v107, v56\n\t"
    "v_add_u32 v104, v104, v56\n\t"
    "v_add_u32 v105, v105, v56\n\t"
    "s_nop 1\n\t"
    "v_mfma_f32_16x16x32_bf16 v[40:43], v[248:251], v[104:107], 0\n\t"
    "v_mfma_f32_16x16x32_bf16 v[44:47], v[252:255], v[104:107], 0\n\t"
    "s_waitcnt vmcnt(3)\n\t"
    ".Lms_%=:\n\t"
    "v_mfma_f32_16x16x32_bf16 v[48:51], %[sel], v[108:111], 0\n\t"
    "v_max3_u32 v60, v108, v109, v110\n\t"
    "v_cmp_u_f32 vcc, v60, v111\n\t"
    "s_nop 1\n\t"
    "s_cbranch_vccnz .Lfs_%=\n\t"
    ".Lsg_%=:\n\t"
    "s_nop 0\n\t"
    "v_cndmask_b32_e64 v40, v40, v42, s[46:47]\n\t"
    "v_cndmask_b32_e64 v41, v41, v43, s[46:47]\n\t"
    "v_max3_f32 v56, v48, v49, v50\n\t"
    "v_max3_f32 v57, v56, v51, v61\n\t"
    "v_max3_f32 v56, v56, v51, v61\n\t"
    "s_nop 1\n\t"
    "v_permlane32_swap_b32 v56, v57\n\t"
    "v_max_f32 v62, v56, v57\n\t"
    "v_sub_f32 v48, v48, v62\n\t"
    "v_sub_f32 v49, v49, v62\n\t"
    "v_exp_f32 v48, v48\n\t"
    "v_exp_f32 v49, v49\n\t"
    "v_sub_f32 v66, v61, v62\n\t"
    "v_cvt_pk_bf16_f32 v63, v48, v49\n\t"
    "v_and_b32 v64, v63, v120\n\t"
    "v_and_b32 v65, v63, v121\n\t"
    "s_nop 1\n\t"
    "v_mfma_f32_16x16x16_bf16 v[52:55], v[78:79], v[64:65], 0\n\t"
    "v_exp_f32 v66, v66\n\t"
    "s_nop 6\n\t"
    "v_fma_f32 v56, v44, v66, v54\n\t"
    "v_rcp_f32 v56, v56\n\t"
    "v_fma_f32 v52, v40, v66, v52\n\t"
    "v_fma_f32 v53, v41, v66, v53\n\t"
    "v_mul_f32 v52, v52, v56\n\t"
    "v_mul_f32 v53, v53, v56\n\t"
    "s_nop 0\n\t"
    "v_add_f32_dpp v52, v52, v52 row_ror:8 row_mask:0xf bank_mask:0xf\n\t"
    "v_add_f32_dpp v53, v53, v53 row_ror:8 row_mask:0xf bank_mask:0xf\n\t"
    "v_cvt_pk_bf16_f32 v56, v52, v53\n\t"
    "ds_write_b32 %[lred], v56\n\t"
    "s_waitcnt lgkmcnt(0)\n\t"
    "s_barrier\n\t"
    "s_waitcnt vmcnt(2)\n\t"
    ".Ltg_%=:\n\t"
    "v_readfirstlane_b32 s96, v122\n\t"
    "s_cmp_eq_u32 s96, -1\n\t"
    "s_cbranch_scc1 .Ltf_%=\n\t"
    ".Lth_%=:\n\t"
    "s_cbranch_execnz .Lloop_%=\n\t"
    "s_branch .Lp2_%=\n\t"
    "s_nop 7\n\t"
    ".Llt_%=:\n\t"
    "s_nop 15\n\t"
    "s_nop 15\n\t"
    "s_branch .Llu_%=\n\t"
    ".Ltf_%=:\n\t"
    "s_mov_b32 s96, s62\n\t"
    "s_sub_u32 %[cap], %[cap], 1\n\t"
    "s_cbranch_scc1 .Ltd_%=\n\t"
    "buffer_load_dword v122, %[a1], %[rs], s45 offen sc1\n\t"
    "s_waitcnt vmcnt(0)\n\t"
    "s_branch .Ltg_%=\n\t"
    ".Ltd_%=:\n\t"
    "s_mov_b32 %[cap], 0\n\t"
    "s_branch .Lth_%=\n\t"
    ".Lfe_%=:\n\t"
    "v_max3_u32 v60, v104, v105, v106\n\t"
    "v_max_u32 v60, v60, v107\n\t"
    "v_cmp_eq_u32 vcc, -1, v60\n\t"
    "s_nop 0\n\t"
    "s_cbranch_vccz .Leg_%=\n\t"
    "ds_write_b32 v112, v113 offset:14336\n\t"
    "s_sub_u32 %[cap], %[cap], 1\n\t"
    "s_cbranch_scc1 .Lde_%=\n\t"
    "buffer_load_dwordx4 v[104:107], %[vl], %[rs], s39 offen sc1\n\t"
    "buffer_load_dwordx4 v[108:111], %[vl], %[rs], s38 offen sc1\n\t"
    "s_waitcnt vmcnt(0)\n\t"
    "s_branch .Lme_%=\n\t"
    ".Lde_%=:\n\t"
    "s_mov_b32 %[cap], 0\n\t"
    "s_branch .Leg_%=\n\t"
    ".Lfs_%=:\n\t"
    "v_max3_u32 v60, v108, v109, v110\n\t"
    "v_max_u32 v60, v60, v111\n\t"
    "v_cmp_eq_u32 vcc, -1, v60\n\t"
    "s_nop 0\n\t"
    "s_cbranch_vccz .Lsg_%=\n\t"
    "ds_write_b32 v112, v113 offset:14336\n\t"
    "s_sub_u32 %[cap], %[cap], 1\n\t"
    "s_cbranch_scc1 .Lds_%=\n\t"
    "buffer_load_dwordx4 v[108:111], %[vl], %[rs], s38 offen sc1\n\t"
    "s_waitcnt vmcnt(0)\n\t"
    "s_branch .Lms_%=\n\t"
    ".Lds_%=:\n\t"
    "s_mov_b32 %[cap], 0\n\t"
    "s_branch .Lsg_%=\n\t"
    ".Lend_%=:\n\t"
    "buffer_store_dword v80, %[vz0], %[rs], s97 offen sc1\n\t"
    "buffer_store_dword v81, %[vz1], %[rs], s97 offen sc1\n\t"
    "s_add_u32 s97, s97, 114688\n\t"
    "s_waitcnt vmcnt(0) lgkmcnt(0)\n\t"
    "s_barrier\n\t"
    "s_mov_b32 m0, s40\n\t"
    "s_setprio 0\n\t"
    "s_lshl_b32 s50, s99, 20\n\t"
    HELP_OPS);
}
DI void role_chain(const Args& a, rsrc_t ws, u32 xb, u32 slot, char* lds) {
  const int w = uni(threadIdx.x >> 6);
  const u32 lb = (u32)(uintptr_t)(LAS char*)lds;
  switch (w) {
    case 0: chain_main<0>(a, xb, slot, lb); break;
    case 1: chain_main<1>(a, xb, slot, lb); break;
    case 2: chain_main<2>(a, xb, slot, lb); break;
    case 3: chain_main<3>(a, xb, slot, lb); break;
    case 4: chain_help<0>(a, ws, xb, slot, lds, lb); break;
    case 5: chain_help<1>(a, ws, xb, slot, lds, lb); break;
    case 6: chain_help<2>(a, ws, xb, slot, lds, lb); break;
    default: chain_help<3>(a, ws, xb, slot, lds, lb); break;
  }
}

DI void role_x(const Args& a, rsrc_t ws, int xc) {
  const int tid = threadIdx.x, lane = tid & 63, w = uni(tid >> 6), r = lane & 15, g = lane >> 4;
  bool dead = false;
  const int h = 2 * (xc & 7) + (w & 1);
  u32 gprog = 0;
  for (int s = (w >> 1) * 3 + (xc >> 3); s < NSEG; s += 4 * 3) {
    const u32 need = ((u32)s >> 1) + 1;
    v4u xf[2][KS];
#pragma unroll
    for (int ks = 0; ks < KS; ++ks)
#pragma unroll
      for (int tt = 0; tt < 2; ++tt) xf[tt][ks] = ld_pl(ws, lane * 16 + (ks & 3) * 1024, OFF_XN + ((s * 2 + tt) * KS + (ks & ~3)) * 1024);
    if (gprog < need) gprog = wait_flags<4>(ws, F_WPROG + (56 + ((14 * h) >> 2)) * 128, need, lane, dead);
#pragma unroll
    for (int ks = 0; ks < KS; ++ks)
#pragma unroll
      for (int tt = 0; tt < 2; ++tt) {
        const auto p = __builtin_amdgcn_permlane16_swap(xf[tt][ks].x, xf[tt][ks].y, false, false);
        const auto t = __builtin_amdgcn_permlane16_swap(xf[tt][ks].z, xf[tt][ks].w, false, false);
        xf[tt][ks] = (v4u){p[0], t[0], p[1], t[1]};
      }
    v4f a00 = {0, 0, 0, 0}, a01 = a00, a11 = a00, am0 = a00, am1 = a00;
#pragma unroll
    for (int kh = 0; kh < 2; ++kh) {
      v4u g0[7], g1[7];
#pragma unroll
      for (int k = 0; k < 7; ++k) {
        g0[k] = ld_dev(ws, lane * 16, OFF_G + ((s * NH + h) * 28 + (kh * 7 + k) * 2) * 1024);
        g1[k] = ld_dev(ws, lane * 16 + 1024, OFF_G + ((s * NH + h) * 28 + (kh * 7 + k) * 2) * 1024);
      }
#pragma unroll
      for (int k = 0; k < 7; ++k) {
        a00 = mfma32(g0[k], xf[0][kh * 7 + k], a00);
        a01 = mfma32(g0[k], xf[1][kh * 7 + k], a01);
        a11 = mfma32(g1[k], xf[1][kh * 7 + k], a11);
      }
    }
    wait_flags<4, true>(ws, F_ZPROG + 4 * h * 128, s < 63 ? (u32)s + 1 : 63u, lane, dead);
    v4u mfr[KS];
    for (u32 it = 0;; ++it) {
      cfence();
      bool un = false;
#pragma unroll
      for (int ks = 0; ks < KS; ++ks) mfr[ks] = ld_dev(ws, g * 128 + (r & 7) * 16 + (ks & 7) * 512, OFF_GM + (s * NH + h) * 7168 + (ks >> 3) * 4096);
      if (s < 63 || dead) break;
#pragma unroll
      for (int ks = 0; ks < KS; ++ks) un |= unset(mfr[ks]);
      if (!any(un)) break;
      if ((it & 63) == 63 && wait_flags<4>(ws, F_ZPROG + 4 * h * 128, 0, lane, dead) == 64) dead = true;
      if (it > SPIN_CAP) give_up(ws, dead);
    }
#pragma unroll
    for (int ks = 0; ks < KS; ++ks) {
      am0 = mfma32(mfr[ks], xf[0][ks], am0);
      am1 = mfma32(mfr[ks], xf[1][ks], am1);
    }
    u32 pm[2][2];
#pragma unroll
    for (int tt = 0; tt < 2; ++tt) {
      v4f lo = tt == 0 ? a00 : a01, hi = tt == 0 ? (v4f){NEGM, NEGM, NEGM, NEGM} : a11, mm = tt == 0 ? am0 : am1;
#pragma unroll
      for (int e = 0; e < 4; ++e) {
        const bool off = 4 * g + e > r;
        if (tt == 0) lo[e] = off ? NEGM : lo[e]; else hi[e] = off ? NEGM : hi[e];
      }
      float m = fmaxf(fmaxf(fmaxf(lo[0], lo[1]), fmaxf(lo[2], lo[3])), fmaxf(fmaxf(hi[0], hi[1]), fmaxf(hi[2], hi[3])));
      m = fmaxf(m, fmaxf(fmaxf(mm[0], mm[1]), fmaxf(mm[2], mm[3])));
      m = xmax32(xmax16(m));
#pragma unroll
      for (int e = 0; e < 4; ++e) { lo[e] = ex2(lo[e] - m); hi[e] = ex2(hi[e] - m); mm[e] = ex2(mm[e] - m); }
      float l = (lo[0] + lo[1]) + (lo[2] + lo[3]) + (hi[0] + hi[1]) + (hi[2] + hi[3]) + 0.5f * ((mm[0] + mm[1]) + (mm[2] + mm[3]));
      l = 1.0f / xadd32(xadd16(l));
      lo *= l; hi *= l; mm *= l;
      st_dev((v4u){pk(lo[0], lo[1]), pk(lo[2], lo[3]), pk(hi[0], hi[1]), pk(hi[2], hi[3])}, ws, lane * 16 + tt * 1024, OFF_PX + (s * NH + h) * 2048);
      pm[tt][0] = pk(mm[0], mm[1]); pm[tt][1] = pk(mm[2], mm[3]);
    }
    const auto t0 = __builtin_amdgcn_permlane16_swap(pm[0][0], pm[1][0], false, false);
    const auto t1 = __builtin_amdgcn_permlane16_swap(pm[0][1], pm[1][1], false, false);
    if (lane < 32) st_dev((v4u){t0[0], t1[0], t0[1], t1[1]}, ws, g * 1024 + ((h & 3) * 16 + r) * 16, OFF_PM + (s * 4 + (h >> 2)) * 2048);
    drain();
    if (lane == 0) __hip_atomic_fetch_add((GAS u32*)(a.ws + F_PCNT + s * 128), 1u, __ATOMIC_RELAXED, __HIP_MEMORY_SCOPE_AGENT);
  }
}

template <int MODE>
DI void ws_gemm(const Args& a, rsrc_t ws, char* lds, const v4u (&wf)[2][KS], int id, int p0, int p1) {
  const int tid = threadIdx.x, lane = tid & 63, w = uni(tid >> 6), g = lane >> 4;
  const int cg = w & 3, th = w >> 2;
  const u32 lbase = (u32)(uintptr_t)(LAS char*)lds;
  constexpr u32 XB = 56 * 1024;
  constexpr int NP = MODE < 2 ? 3 : 1;
  const char* xsrc = a.ws + OFF_XN + w * 7 * 1024 + lane * 16;
#pragma unroll
  for (int i = 0; i < 7; ++i) glds16(xsrc + (size_t)p0 * XB + i * 1024, lbase + (p0 & 1) * XB + (w * 7 + i) * 1024);
  drain();
  for (int p = p0; p < p1; ++p) {
    bar();
    if (p > p0 && tid == 0) {
      const int done = p <= p0 + NP ? p : p - 1;
      if (MODE < 2) { if (p != p0 + NP + 1) st_dev1((u32)done, ws, F_WPROG + id * 128, 0); }
      else if (p != p0 + 2) __hip_atomic_fetch_add((GAS u32*)(a.ws + F_HCNT + (done - 1) * 128), 1u, __ATOMIC_RELAXED, __HIP_MEMORY_SCOPE_AGENT);
    }
    if (p + 1 < p1) {
#pragma unroll
      for (int i = 0; i < 7; ++i) glds16(xsrc + (size_t)(p + 1) * XB + i * 1024, lbase + ((p + 1) & 1) * XB + (w * 7 + i) * 1024);
    }
    v4f acc[2][2];
#pragma unroll
    for (int c = 0; c < 2; ++c)
#pragma unroll
      for (int t = 0; t < 2; ++t) acc[c][t] = (v4f){0, 0, 0, 0};
    const u32 xo = (p & 1) * XB + th * 28 * 1024 + lane * 16;
#pragma unroll
    for (int ks = 0; ks < KS; ++ks) {
      const v4u x0 = lds_r128(lds, xo + ks * 1024), x1 = lds_r128(lds, xo + (KS + ks) * 1024);
#pragma unroll
      for (int c = 0; c < 2; ++c) {
        if (MODE == 0) { acc[c][0] = mfma32(x0, wf[c][ks], acc[c][0]); acc[c][1] = mfma32(x1, wf[c][ks], acc[c][1]); }
        else { acc[c][0] = mfma32(wf[c][ks], x0, acc[c][0]); acc[c][1] = mfma32(wf[c][ks], x1, acc[c][1]); }
      }
    }
    const int s = 2 * p + th;
    if (MODE == 0) {
#pragma unroll
      for (int c = 0; c < 2; ++c) {
        const int src = ((lane & 48) + (lane & 2) * 4 + ((lane & 15) >> 2) * 2 + (lane & 1)) * 4;
        auto bp = [&](float x, float y) { return (u32)__builtin_amdgcn_ds_bpermute(src, (int)pk(x, y)); };
        st_dev((v4u){bp(acc[c][0][0], acc[c][0][1]), bp(acc[c][0][2], acc[c][0][3]), bp(acc[c][1][0], acc[c][1][1]), bp(acc[c][1][2], acc[c][1][3])},
               ws, lane * 16 + c * 1024, OFF_U + ((s * 56 + id) * 8 + 2 * cg) * 1024);
      }
    } else if (MODE == 1) {
#pragma unroll
      for (int t = 0; t < 2; ++t)
        st_dev((v4u){pk(acc[0][t][0], acc[0][t][1]), pk(acc[0][t][2], acc[0][t][3]), pk(acc[1][t][0], acc[1][t][1]), pk(acc[1][t][2], acc[1][t][3])},
               ws, lane * 16 + t * 1024, OFF_G + (s * 224 + (id - 56) * 4 + cg) * 2048);
    } else {
      u32 hp[2][2];
#pragma unroll
      for (int t = 0; t < 2; ++t) {
        float v[4];
#pragma unroll
        for (int e = 0; e < 4; ++e) v[e] = rbf(silu(rbf(acc[0][t][e]))) * rbf(acc[1][t][e]);
        hp[t][0] = pk(v[0], v[1]); hp[t][1] = pk(v[2], v[3]);
      }
      const auto t0 = __builtin_amdgcn_permlane16_swap(hp[0][0], hp[1][0], false, false);
      const auto t1 = __builtin_amdgcn_permlane16_swap(hp[0][1], hp[1][1], false, false);
      st_dev((v4u){t0[0], t1[0], t0[1], t1[1]}, ws, (g & 1) * 56 * 1024 + ((2 * (cg & 1) + (g >> 1)) * 16 + (lane & 15)) * 16,
             OFF_H + (s * 2 * 56 + 2 * id + (cg >> 1)) * 1024);
    }
    if (p < p0 + NP || p + 1 == p1) drain();
    else if (MODE < 2) asm volatile("s_waitcnt vmcnt(2)" ::: "memory");
    else asm volatile("s_waitcnt vmcnt(1)" ::: "memory");
  }
  bar();
  if (tid == 0) {
    if (MODE < 2) st_dev1((u32)p1, ws, F_WPROG + id * 128, 0);
    else {
      if (p1 - p0 > 2) __hip_atomic_fetch_add((GAS u32*)(a.ws + F_HCNT + (p1 - 2) * 128), 1u, __ATOMIC_RELAXED, __HIP_MEMORY_SCOPE_AGENT);
      __hip_atomic_fetch_add((GAS u32*)(a.ws + F_HCNT + (p1 - 1) * 128), 1u, __ATOMIC_RELAXED, __HIP_MEMORY_SCOPE_AGENT);
    }
  }
}

DI void worker_front(const Args& a, rsrc_t ws, int id, char* lds) {
  const int tid = threadIdx.x, lane = tid & 63, w = uni(tid >> 6);
  const int cg = w & 3;
  v4u wf[2][KS];
  if (id < 112) {
    const u32 lo = id < 56 ? (lane & 8) * 64 + (lane >> 4) * 128 + (lane & 7) * 16 : lane * 16;
#pragma unroll
    for (int c = 0; c < 2; ++c)
#pragma unroll
      for (int ks = 0; ks < KS; ++ks) wf[c][ks] = ld_pl(ws, lo + (ks & 3) * 1024, OFF_WB + id * WBSZ + ((2 * cg + c) * KS + (ks & ~3)) * 1024);
    if (id < 56) ws_gemm<0>(a, ws, lds, wf, id, 0, 32);
    else ws_gemm<1>(a, ws, lds, wf, id, 0, 32);
  } else {
    const int fb = (id - 112) >> 1, hf = id & 1;
    for (int i = 0; i < 3; ++i) __builtin_amdgcn_s_sleep(40);
    stage64(a.w1 + fb * 64, a.w3 + fb * 64, FF, lds, tid);
    bar();
#pragma unroll
    for (int c = 0; c < 2; ++c)
#pragma unroll
      for (int ks = 0; ks < KS; ++ks) wf[c][ks] = tr_frag(lds, c * SIMG, SP, ks, lane, cg * 32 + (lane & 3) * 8);
    bar();
    ws_gemm<2>(a, ws, lds, wf, fb, 16 * hf, 16 * hf + 16);
  }
}

DI void worker_back(const Args& a, rsrc_t ws, int id, char* lds) {
  const int tid = threadIdx.x, lane = tid & 63, w = uni(tid >> 6), r = lane & 15, g = lane >> 4;
  bool dead = false;
  const int cb = id % 14, r12 = id / 14;
  v4u w2f[2][7];
  {
    constexpr u32 P2 = 80;
    v4u t[14];
#pragma unroll
    for (int i = 0; i < 14; ++i) t[i] = *(const GAS v4u*)(a.w2 + (size_t)(w * 224 + i * 16 + (lane >> 2)) * DM + cb * 32 + (lane & 3) * 8);
#pragma unroll
    for (int i = 0; i < 14; ++i) lds_w128(lds, (w * 224 + i * 16 + (lane >> 2)) * P2 + (lane & 3) * 16, t[i]);
    asm volatile("s_waitcnt lgkmcnt(0)" ::: "memory");
#pragma unroll
    for (int c = 0; c < 2; ++c)
#pragma unroll
      for (int k = 0; k < 7; ++k) w2f[c][k] = tr_frag(lds, w * 224 * P2, P2, k, lane, (lane & 3) * 16 + c * 8);
  }
  u32 uprog = 0;
  const u32 ln = 2 * (r >> 3) + ((r >> 1) & 1), tc = 8 * ((r >> 2) & 1) + (r & 1);
  const u32 vo_u = ln * 8192 + (tc + 16 * g) * 16;
  const u32 vo_z = ln * 2048 + (g >> 1) * 256 + (tc + 2 * (g & 1)) * 16;
  const int ot = w & 1, oc = (w >> 1) & 1, orp = w >> 2;
  for (int s = r12; s < NSEG; s += 12) {
    if (w == 0) {
      if (uprog <= (u32)s >> 1) uprog = wait_flags<4, true>(ws, F_WPROG + 4 * cb * 128, ((u32)s >> 1) + 1, lane, dead);
      wait_flags<1>(ws, F_HCNT + (s >> 1) * 128, 28, lane, dead);
    }
    bar();
    v4u hf[2][7], uf[2][2];
#pragma unroll
    for (int t = 0; t < 2; ++t)
#pragma unroll
      for (int k = 0; k < 7; ++k) hf[t][k] = ld_dev(ws, lane * 16 + (k & 3) * 1024, OFF_H + ((s * 2 + t) * 56 + w * 7 + (k & 4)) * 1024);
#pragma unroll
    for (int hh = 0; hh < 2; ++hh)
#pragma unroll
      for (int c = 0; c < 2; ++c) uf[hh][c] = ld_dev(ws, vo_u + (hh * 2 + 4 * c) * 16, OFF_U + ((s * 56 + 4 * cb) * 8 + w) * 1024);
    const size_t oidx = (size_t)(s * 32 + ot * 16 + r) * DM + cb * 32 + g * 8 + oc * 4 + orp * 2;
    const u32 xin = *(const GAS u32*)(a.x + oidx);
    v4f ag[2][2], as[2][2];
#pragma unroll
    for (int c = 0; c < 2; ++c)
#pragma unroll
      for (int t = 0; t < 2; ++t) { ag[c][t] = (v4f){0, 0, 0, 0}; as[c][t] = (v4f){0, 0, 0, 0}; }
#pragma unroll
    for (int k = 0; k < 7; ++k)
#pragma unroll
      for (int c = 0; c < 2; ++c)
#pragma unroll
        for (int t = 0; t < 2; ++t) ag[c][t] = mfma32(w2f[c][k], hf[t][k], ag[c][t]);
#pragma unroll
    for (int c = 0; c < 2; ++c)
#pragma unroll
      for (int t = 0; t < 2; ++t) lds_w128(lds, ((w * 8 + c * 2 + t) * 64 + lane) * 16, f2u(ag[c][t]));
    if (w == 0) {
      wait_flags<4, true>(ws, F_ZPROG + (4 * cb + (cb >= 7 ? 4 : 0)) * 128, s < 63 ? (u32)s + 1 : 63u, lane, dead);
      if (s < 62) wait_flags<1>(ws, F_PCNT + s * 128, 16, lane, dead);
    }
    bar();
    v4u pf[2][2], zf[2], pmf;
    for (u32 it = 0;; ++it) {
      cfence();
#pragma unroll
    for (int hh = 0; hh < 2; ++hh)
#pragma unroll
      for (int t = 0; t < 2; ++t) pf[hh][t] = ld_dev(ws, lane * 16 + (hh * 2 + t) * 1024, OFF_PX + (s * NH + 2 * w) * 2048);
#pragma unroll
    for (int c = 0; c < 2; ++c) zf[c] = ld_dev(ws, vo_z + c * 64, OFF_Z + (s * 56 + 4 * cb) * 2048 + (w >> 1) * 512);
    pmf = ld_dev(ws, lane * 16, OFF_PM + (s * 8 + w) * 1024);
      if (s < 62 || dead) break;
      if (!any(unset(pf[0][0]) | unset(pf[0][1]) | unset(pf[1][0]) | unset(pf[1][1]) | unset(zf[0]) | unset(zf[1]) | unset(pmf))) break;
      if ((it & 63) == 63 && wait_flags<1>(ws, F_PCNT + s * 128, 0, lane, dead) == 16 && wait_flags<4>(ws, F_ZPROG + (4 * cb + (cb >= 7 ? 4 : 0)) * 128, 0, lane, dead) == 64) dead = true;
      if (it > SPIN_CAP) give_up(ws, dead);
    }
    v2f gs = {0, 0};
#pragma unroll
    for (int v = 0; v < 8; ++v) gs += __builtin_bit_cast(v2f, lds_r64(lds, ((v * 8 + oc * 2 + ot) * 64 + lane) * 16 + orp * 8));
    const float g0 = rbf(silu(rbf(gs[0]))), g1 = rbf(silu(rbf(gs[1])));
#pragma unroll
    for (int hh = 0; hh < 2; ++hh)
#pragma unroll
      for (int c = 0; c < 2; ++c)
#pragma unroll
        for (int t = 0; t < 2; ++t) as[c][t] = mfma32(uf[hh][c], pf[hh][t], as[c][t]);
#pragma unroll
    for (int c = 0; c < 2; ++c) {
      if (w & 1) as[c][1] = mfma32(zf[c], pmf, as[c][1]);
      else as[c][0] = mfma32(zf[c], pmf, as[c][0]);
    }
#pragma unroll
    for (int c = 0; c < 2; ++c)
#pragma unroll
      for (int t = 0; t < 2; ++t) lds_w128(lds, ((w * 8 + 4 + c * 2 + t) * 64 + lane) * 16, f2u(as[c][t]));
    bar();
    v2f ss = {0, 0};
#pragma unroll
    for (int v = 0; v < 8; ++v) ss += __builtin_bit_cast(v2f, lds_r64(lds, ((v * 8 + 4 + oc * 2 + ot) * 64 + lane) * 16 + orp * 8));
    *(GAS u32*)(a.out + oidx) = pk(bflo(xin) + rbf(rbf(ss[0]) * g0), bfhi(xin) + rbf(rbf(ss[1]) * g1));
  }
}

__global__ void __launch_bounds__(512, 2) k_main(Args a) {
  extern __shared__ __attribute__((aligned(16))) char lds[];
  const rsrc_t ws = __builtin_amdgcn_make_buffer_rsrc(a.ws, 0, WS_END, 0x00020000);
  const int b = blockIdx.x, lab = b & 7, slot = b >> 3;
  if ((lab & 5) == 0) { role_chain(a, ws, lab >> 1, slot, lds); }
  else if (lab == 1 && slot < 24) { if (slot < 8 * 3) role_x(a, ws, slot); }
  else {
    const int id = lab == 1 ? slot - 24 : 8 + (lab - 3) * 32 + slot;
    worker_front(a, ws, id, lds);
    { bar(); worker_back(a, ws, id, lds); }
  }
}

}

extern "C" void kernel_launch(void* const* d_in, const int* in_sizes, int n_in, void* d_out, int out_size, void* d_ws,
                              size_t ws_size, hipStream_t stream) {
  Args a;
  a.x = (const u16*)d_in[0]; a.mem0 = (const u16*)d_in[1]; a.wq = (const u16*)d_in[2]; a.wk = (const u16*)d_in[3];
  a.wv = (const u16*)d_in[4]; a.wo = (const u16*)d_in[5]; a.w1 = (const u16*)d_in[8]; a.w3 = (const u16*)d_in[9];
  a.w2 = (const u16*)d_in[10]; a.out = (u16*)d_out;
  a.ws = (char*)(((uintptr_t)d_ws + ((2u << 20) - 1)) & ~(uintptr_t)((2u << 20) - 1));
  (void)hipFuncSetAttribute((const void*)k_prep, hipFuncAttributeMaxDynamicSharedMemorySize, LDS_PREP);
  (void)hipFuncSetAttribute((const void*)k_main, hipFuncAttributeMaxDynamicSharedMemorySize, LDS_MAIN);
  k_prep<<<256, 512, LDS_PREP, stream>>>(a);
  k_main<<<256, 512, LDS_MAIN, stream>>>(a);
}
```
